```python
import torch

def _softmax(x, axis=-1):
    return torch.softmax(x, dim=axis)
def _gelu(x):
    return torch.nn.functional.gelu(x, approximate="tanh")
def _silu(x):
    return torch.nn.functional.silu(x)
def _sigmoid(x):
    return torch.sigmoid(x)
def _layernorm(x, axis=-1, eps=1e-5):
    m = x.mean(dim=axis, keepdim=True)
    v = x.var(dim=axis, keepdim=True, unbiased=False)
    return (x - m) / torch.sqrt(v + eps)
def _scatter_add(tgt, idx, src, axis=0):
    return tgt.clone().index_add_(axis, idx, src)
def _segment_sum(src, seg, n):
    out = torch.zeros((n,) + src.shape[1:], dtype=src.dtype)
    return out.index_add_(0, seg, src)
def _pad(x, pw):
    flat = tuple(v for pair in reversed(pw) for v in pair)
    return torch.nn.functional.pad(x, flat)

kernel_name = 'gather_scatter__cheap_equivariant_egnn__graph'

def setup_inputs():

    _w = (lambda a: (lambda t: t.to(torch.bfloat16) if t.is_floating_point() else t)(torch.as_tensor(a)))
    N, deg, D, De = 10000, 16, 128, max(4, 1)
    E = N * deg
    return {
        "h": _w((lambda *s: torch.randn(*s, dtype=torch.bfloat16) * 0.2)(N, D)),
        "pos": _w((lambda *s: torch.randn(*s, dtype=torch.bfloat16) * 0.8)(N, 3)),
        "e_attr": _w((lambda *s: torch.randn(*s, dtype=torch.bfloat16) * 0.8)(E, De)),
        "src": _w((lambda lo, hi, *s: torch.randint(lo, hi, s))(0, N, E).to(torch.int64)),
        "dst": _w((lambda lo, hi, *s: torch.randint(lo, hi, s))(0, N, E).to(torch.int64)),
        "w_e": _w(0.1 * (lambda *s: torch.randn(*s, dtype=torch.bfloat16) * 0.8)(2 * D + 1 + De, D)),
        "w_x": _w(0.1 * (lambda *s: torch.randn(*s, dtype=torch.bfloat16) * 0.2)(D, 1)),
        "w_h": _w(0.1 * (lambda *s: torch.randn(*s, dtype=torch.bfloat16) * 0.2)(2 * D, D)),
    }

def reference(h, pos, e_attr, src, dst, w_e, w_x, w_h):
    N, D = h.shape[0], h.shape[1]
    diff = pos[dst] - pos[src]
    dist2 = torch.sum(diff * diff, axis=-1, keepdims=True)
    e_in = torch.concatenate([h[dst], h[src], dist2, e_attr], axis=-1)
    m_ij = _silu(e_in @ w_e)
    coord_scale = m_ij @ w_x
    pos_new = pos + _segment_sum(diff * coord_scale, dst, N)
    m_i = _segment_sum(m_ij, dst, N)
    h_new = h + _silu(torch.concatenate([h, m_i], axis=-1) @ w_h)
    return h_new, pos_new
```

```cpp
#include <hip/hip_runtime.h>
#include <hip/hip_bf16.h>
#include <stdint.h>

typedef __attribute__((ext_vector_type(8))) short bf16x8;
typedef __attribute__((ext_vector_type(4))) float f32x4;
typedef __attribute__((ext_vector_type(4))) unsigned u32x4;
typedef __attribute__((ext_vector_type(2))) unsigned u32x2;

static constexpr int D = 128;
static constexpr int CAP = 64;

__device__ __forceinline__ float bf_lo(unsigned u) { return __uint_as_float(u << 16); }
__device__ __forceinline__ float bf_hi(unsigned u) { return __uint_as_float(u & 0xffff0000u); }
__device__ __forceinline__ float bf1(unsigned short u) { return __uint_as_float(((unsigned)u) << 16); }
__device__ __forceinline__ unsigned f2bf(float f) {
    __hip_bfloat16 b = __float2bfloat16(f);
    return (unsigned)*reinterpret_cast<unsigned short*>(&b);
}
__device__ __forceinline__ float rbf(float f) { return __uint_as_float(f2bf(f) << 16); }
__device__ __forceinline__ unsigned pack2(float lo, float hi) { return f2bf(lo) | (f2bf(hi) << 16); }

__device__ __forceinline__ float silu_f(float x) {
    float e = __builtin_amdgcn_exp2f(x * -1.44269504088896340736f);
    return x * __builtin_amdgcn_rcpf(1.0f + e);
}

__device__ __forceinline__ int perm_ch(int nt, int i) { return (i >> 2) * 32 + nt * 4 + (i & 3); }

__global__ __launch_bounds__(256) void k0_prep(const unsigned short* __restrict__ w_e,
                                               const unsigned short* __restrict__ w_h,
                                               int* __restrict__ cnt, u32x4* __restrict__ frags, int N) {
    int tid = blockIdx.x * 256 + threadIdx.x;
    int nthreads = gridDim.x * 256;
    for (int i = tid; i < N; i += nthreads) cnt[i] = 0;
    if (tid < 128 * 64) {
        int f = tid >> 6, l = tid & 63;
        const unsigned short* W;
        int nt, ks;
        if (f < 64) { W = w_e + (f >> 5) * 128 * D; nt = (f & 31) >> 2; ks = f & 3; }
        else { int g = f - 64; W = w_h; nt = g >> 3; ks = g & 7; }
        int c = perm_ch(nt, l & 15);
        int k0 = ks * 32 + 8 * (l >> 4);
        unsigned v[8];
#pragma unroll
        for (int j = 0; j < 8; ++j) v[j] = W[(size_t)(k0 + j) * D + c];
        u32x4 o;
        o.x = v[0] | (v[1] << 16); o.y = v[2] | (v[3] << 16); o.z = v[4] | (v[5] << 16); o.w = v[6] | (v[7] << 16);
        frags[tid] = o;
    }
}

template <bool BF16B>
__global__ __launch_bounds__(256) void k1_bin_gemm(const unsigned short* __restrict__ h,
                                                   const u32x2* __restrict__ e_attr,
                                                   const int* __restrict__ src, const int* __restrict__ dst,
                                                   int* __restrict__ cnt, u32x4* __restrict__ bins,
                                                   const u32x4* __restrict__ frags, void* __restrict__ Bt,
                                                   int N, int E, int nbin) {
    if ((int)blockIdx.x < nbin) {
        int e = blockIdx.x * 256 + threadIdx.x;
        if (e < E) {
            int d = dst[e];
            int s = src[e];
            u32x2 ea = e_attr[e];
            int r = atomicAdd(&cnt[d], 1);
            if (r < CAP) {
                u32x4 rec; rec.x = (unsigned)e; rec.y = (unsigned)s; rec.z = ea.x; rec.w = ea.y;
                bins[(size_t)d * CAP + r] = rec;
            }
        }
        return;
    }
    int wave = threadIdx.x >> 6, l = threadIdx.x & 63;
    int n0 = ((int)blockIdx.x - nbin) * 64 + wave * 16;
    if (n0 >= N) return;
    int node = n0 + (l & 15);
    int nodec = node < N ? node : N - 1;
    const u32x4* hrow = (const u32x4*)(h + (size_t)nodec * D);
    bf16x8 hb[4];
#pragma unroll
    for (int ks = 0; ks < 4; ++ks) {
        u32x4 t = hrow[ks * 4 + (l >> 4)];
        hb[ks] = __builtin_bit_cast(bf16x8, t);
    }
    const u32x4* w2 = frags + 32 * 64;
    f32x4 acc[8];
#pragma unroll
    for (int nt = 0; nt < 8; ++nt) {
        acc[nt] = (f32x4){0.f, 0.f, 0.f, 0.f};
#pragma unroll
        for (int ks = 0; ks < 4; ++ks) {
            u32x4 t = w2[(nt * 4 + ks) * 64 + l];
            acc[nt] = __builtin_amdgcn_mfma_f32_16x16x32_bf16(__builtin_bit_cast(bf16x8, t), hb[ks], acc[nt], 0, 0, 0);
        }
    }
    if (node < N) {
        if (BF16B) {
            u32x4* o = (u32x4*)((unsigned short*)Bt + (size_t)node * D + (l >> 4) * 32);
#pragma unroll
            for (int p = 0; p < 4; ++p) {
                u32x4 v;
                v.x = pack2(acc[2 * p][0], acc[2 * p][1]); v.y = pack2(acc[2 * p][2], acc[2 * p][3]);
                v.z = pack2(acc[2 * p + 1][0], acc[2 * p + 1][1]); v.w = pack2(acc[2 * p + 1][2], acc[2 * p + 1][3]);
                o[p] = v;
            }
        } else {
            f32x4* o = (f32x4*)((float*)Bt + (size_t)node * D + (l >> 4) * 32);
#pragma unroll
            for (int nt = 0; nt < 8; ++nt) o[nt] = acc[nt];
        }
    }
}

template <int T, int NW, bool BF16B>
__global__ __launch_bounds__(NW * 64) void k2_agg(const unsigned short* __restrict__ h,
                                                  const unsigned short* __restrict__ pos,
                                                  const u32x2* __restrict__ e_attr,
                                                  const int* __restrict__ src, const int* __restrict__ dst,
                                                  const unsigned short* __restrict__ w_e,
                                                  const unsigned short* __restrict__ w_x,
                                                  const int* __restrict__ cnt, const u32x4* __restrict__ bins,
                                                  const u32x4* __restrict__ frags, const void* __restrict__ Bt,
                                                  unsigned short* __restrict__ out_h, unsigned short* __restrict__ out_pos,
                                                  int N, int E) {
    constexpr int MT = (T + 15) / 16;
    constexpr int AST = D + 4;
    constexpr int MST = D + 8;
    __shared__ __attribute__((aligned(16))) float sA[MT * 16 * AST];
    __shared__ __attribute__((aligned(16))) unsigned short sM[MT * 16 * MST];
    __shared__ __attribute__((aligned(16))) u32x4 sRecA[T * CAP];
    __shared__ __attribute__((aligned(16))) unsigned sRecB[T * CAP];

    const int wave = __builtin_amdgcn_readfirstlane(threadIdx.x >> 6);
    const int l = threadIdx.x & 63;
    const int lg = l & 15, grp = l >> 4;
    const int n0 = blockIdx.x * T;

    for (int task = wave; task < MT * 4; task += NW) {
        int mt = task >> 2, np = task & 3;
        int node = n0 + mt * 16 + lg;
        int nodec = node < N ? node : N - 1;
        const u32x4* hrow = (const u32x4*)(h + (size_t)nodec * D);
        f32x4 a0 = {0.f, 0.f, 0.f, 0.f}, a1 = {0.f, 0.f, 0.f, 0.f};
#pragma unroll
        for (int ks = 0; ks < 4; ++ks) {
            bf16x8 hb = __builtin_bit_cast(bf16x8, hrow[ks * 4 + grp]);
            bf16x8 w0 = __builtin_bit_cast(bf16x8, frags[((np * 2) * 4 + ks) * 64 + l]);
            bf16x8 w1 = __builtin_bit_cast(bf16x8, frags[((np * 2 + 1) * 4 + ks) * 64 + l]);
            a0 = __builtin_amdgcn_mfma_f32_16x16x32_bf16(w0, hb, a0, 0, 0, 0);
            a1 = __builtin_amdgcn_mfma_f32_16x16x32_bf16(w1, hb, a1, 0, 0, 0);
        }
        float* dstp = &sA[(mt * 16 + lg) * AST + grp * 32 + np * 8];
        *(f32x4*)dstp = a0;
        *(f32x4*)(dstp + 4) = a1;
    }

    for (int t = wave; t < T; t += NW) {
        int node = n0 + t;
        if (node >= N) break;
        int c = cnt[node];
        if (c > CAP) continue;
        u32x4 rec = {0xffffffffu, 0u, 0u, 0u};
        if (l < c) rec = bins[(size_t)node * CAP + l];
        float pd0 = bf1(pos[node * 3 + 0]), pd1 = bf1(pos[node * 3 + 1]), pd2 = bf1(pos[node * 3 + 2]);
        int rank = 0;
        for (int j = 0; j < c; ++j) {
            unsigned ej = __builtin_amdgcn_readlane(rec.x, j);
            rank += (ej < rec.x) ? 1 : 0;
        }
        if (l < c) {
            int s = (int)rec.y;
            float d0 = rbf(pd0 - bf1(pos[s * 3 + 0]));
            float d1 = rbf(pd1 - bf1(pos[s * 3 + 1]));
            float d2 = rbf(pd2 - bf1(pos[s * 3 + 2]));
            float q = rbf(rbf(d0 * d0) + rbf(d1 * d1) + rbf(d2 * d2));
            u32x4 ra; ra.x = rec.y; ra.y = rec.z; ra.z = rec.w;
            ra.w = (__float_as_uint(d0) >> 16) | (__float_as_uint(d1) & 0xffff0000u);
            sRecA[t * CAP + rank] = ra;
            sRecB[t * CAP + rank] = (__float_as_uint(d2) >> 16) | (__float_as_uint(q) & 0xffff0000u);
        }
    }

    float wd[8], we[4][8], wx[8];
    {
        u32x4 t = *(const u32x4*)(w_e + (size_t)256 * D + lg * 8);
        wd[0] = bf_lo(t.x); wd[1] = bf_hi(t.x); wd[2] = bf_lo(t.y); wd[3] = bf_hi(t.y);
        wd[4] = bf_lo(t.z); wd[5] = bf_hi(t.z); wd[6] = bf_lo(t.w); wd[7] = bf_hi(t.w);
#pragma unroll
        for (int k = 0; k < 4; ++k) {
            t = *(const u32x4*)(w_e + (size_t)(257 + k) * D + lg * 8);
            we[k][0] = bf_lo(t.x); we[k][1] = bf_hi(t.x); we[k][2] = bf_lo(t.y); we[k][3] = bf_hi(t.y);
            we[k][4] = bf_lo(t.z); we[k][5] = bf_hi(t.z); we[k][6] = bf_lo(t.w); we[k][7] = bf_hi(t.w);
        }
        t = *(const u32x4*)(w_x + lg * 8);
        wx[0] = bf_lo(t.x); wx[1] = bf_hi(t.x); wx[2] = bf_lo(t.y); wx[3] = bf_hi(t.y);
        wx[4] = bf_lo(t.z); wx[5] = bf_hi(t.z); wx[6] = bf_lo(t.w); wx[7] = bf_hi(t.w);
    }
    __syncthreads();

    for (int t = wave; t < T; t += NW) {
        int node = n0 + t;
        if (node >= N) break;
        int ctot = cnt[node];
        float A[8];
        {
            f32x4 a0 = *(const f32x4*)&sA[t * AST + lg * 8];
            f32x4 a1 = *(const f32x4*)&sA[t * AST + lg * 8 + 4];
            A[0] = a0[0]; A[1] = a0[1]; A[2] = a0[2]; A[3] = a0[3];
            A[4] = a1[0]; A[5] = a1[1]; A[6] = a1[2]; A[7] = a1[3];
        }
        float m[8], P0[8], P1[8], P2[8];
#pragma unroll
        for (int j = 0; j < 8; ++j) { m[j] = 0.f; P0[j] = 0.f; P1[j] = 0.f; P2[j] = 0.f; }

        const bool fallback = ctot > CAP;
        int nchunks = fallback ? (E + 63) / 64 : 1;
        float pd0 = 0.f, pd1 = 0.f, pd2 = 0.f;
        if (fallback) { pd0 = bf1(pos[node * 3 + 0]); pd1 = bf1(pos[node * 3 + 1]); pd2 = bf1(pos[node * 3 + 2]); }
        for (int ch = 0; ch < nchunks; ++ch) {
            int c = ctot;
            if (fallback) {
                int e = ch * 64 + l;
                bool hit = (e < E) && (dst[e] == node);
                unsigned long long mask = __ballot(hit);
                c = __popcll(mask);
                if (c == 0) continue;
                __builtin_amdgcn_wave_barrier();
                if (hit) {
                    int rank = __popcll(mask & ((1ull << l) - 1ull));
                    int s = src[e];
                    u32x2 ea = e_attr[e];
                    float d0 = rbf(pd0 - bf1(pos[s * 3 + 0]));
                    float d1 = rbf(pd1 - bf1(pos[s * 3 + 1]));
                    float d2 = rbf(pd2 - bf1(pos[s * 3 + 2]));
                    float q = rbf(rbf(d0 * d0) + rbf(d1 * d1) + rbf(d2 * d2));
                    u32x4 ra; ra.x = (unsigned)s; ra.y = ea.x; ra.z = ea.y;
                    ra.w = (__float_as_uint(d0) >> 16) | (__float_as_uint(d1) & 0xffff0000u);
                    sRecA[t * CAP + rank] = ra;
                    sRecB[t * CAP + rank] = (__float_as_uint(d2) >> 16) | (__float_as_uint(q) & 0xffff0000u);
                }
                __builtin_amdgcn_wave_barrier();
            }
            for (int i = grp; i < c; i += 4) {
                u32x4 ra = sRecA[t * CAP + i];
                unsigned rb = sRecB[t * CAP + i];
                float b[8];
                if (BF16B) {
                    u32x4 bv = *(const u32x4*)((const unsigned short*)Bt + (size_t)ra.x * D + lg * 8);
                    b[0] = bf_lo(bv.x); b[1] = bf_hi(bv.x); b[2] = bf_lo(bv.y); b[3] = bf_hi(bv.y);
                    b[4] = bf_lo(bv.z); b[5] = bf_hi(bv.z); b[6] = bf_lo(bv.w); b[7] = bf_hi(bv.w);
                } else {
                    f32x4 b0 = *(const f32x4*)((const float*)Bt + (size_t)ra.x * D + lg * 8);
                    f32x4 b1 = *(const f32x4*)((const float*)Bt + (size_t)ra.x * D + lg * 8 + 4);
                    b[0] = b0[0]; b[1] = b0[1]; b[2] = b0[2]; b[3] = b0[3];
                    b[4] = b1[0]; b[5] = b1[1]; b[6] = b1[2]; b[7] = b1[3];
                }
                float ea0 = bf_lo(ra.y), ea1 = bf_hi(ra.y), ea2 = bf_lo(ra.z), ea3 = bf_hi(ra.z);
                float d0 = bf_lo(ra.w), d1 = bf_hi(ra.w), d2 = bf_lo(rb), q = bf_hi(rb);
#pragma unroll
                for (int j = 0; j < 8; ++j) {
                    float x = A[j] + b[j];
                    x = fmaf(q, wd[j], x);
                    x = fmaf(ea0, we[0][j], x);
                    x = fmaf(ea1, we[1][j], x);
                    x = fmaf(ea2, we[2][j], x);
                    x = fmaf(ea3, we[3][j], x);
                    float s = silu_f(x);
                    m[j] += s;
                    P0[j] = fmaf(d0, s, P0[j]);
                    P1[j] = fmaf(d1, s, P1[j]);
                    P2[j] = fmaf(d2, s, P2[j]);
                }
            }
        }
        float p0 = 0.f, p1 = 0.f, p2 = 0.f;
#pragma unroll
        for (int j = 0; j < 8; ++j) {
            p0 = fmaf(P0[j], wx[j], p0); p1 = fmaf(P1[j], wx[j], p1); p2 = fmaf(P2[j], wx[j], p2);
            m[j] += __shfl_xor(m[j], 16);
            m[j] += __shfl_xor(m[j], 32);
        }
#pragma unroll
        for (int o = 1; o < 64; o <<= 1) {
            p0 += __shfl_xor(p0, o); p1 += __shfl_xor(p1, o); p2 += __shfl_xor(p2, o);
        }
        if (grp == 0) {
            u32x4 v;
            v.x = pack2(m[0], m[1]); v.y = pack2(m[2], m[3]); v.z = pack2(m[4], m[5]); v.w = pack2(m[6], m[7]);
            *(u32x4*)&sM[t * MST + lg * 8] = v;
        }
        if (l < 3) {
            float p = l == 0 ? p0 : (l == 1 ? p1 : p2);
            out_pos[(size_t)node * 3 + l] = (unsigned short)f2bf(bf1(pos[node * 3 + l]) + p);
        }
    }
    __syncthreads();

    const u32x4* wh = frags + 64 * 64;
    for (int task = wave; task < MT * 4; task += NW) {
        int mt = task >> 2, np = task & 3;
        int row = mt * 16 + lg;
        int node = n0 + row;
        bool valid = (row < T) && (node < N);
        int nodec = node < N ? node : N - 1;
        const u32x4* hrow = (const u32x4*)(h + (size_t)nodec * D);
        f32x4 a0 = {0.f, 0.f, 0.f, 0.f}, a1 = {0.f, 0.f, 0.f, 0.f};
#pragma unroll
        for (int ks = 0; ks < 8; ++ks) {
            bf16x8 xb;
            if (ks < 4) xb = __builtin_bit_cast(bf16x8, hrow[ks * 4 + grp]);
            else xb = *(const bf16x8*)&sM[row * MST + (ks - 4) * 32 + grp * 8];
            bf16x8 w0 = __builtin_bit_cast(bf16x8, wh[((np * 2) * 8 + ks) * 64 + l]);
            bf16x8 w1 = __builtin_bit_cast(bf16x8, wh[((np * 2 + 1) * 8 + ks) * 64 + l]);
            a0 = __builtin_amdgcn_mfma_f32_16x16x32_bf16(w0, xb, a0, 0, 0, 0);
            a1 = __builtin_amdgcn_mfma_f32_16x16x32_bf16(w1, xb, a1, 0, 0, 0);
        }
        if (valid) {
            u32x4 hv = hrow[grp * 4 + np];
            u32x4 v;
            v.x = pack2(bf_lo(hv.x) + silu_f(a0[0]), bf_hi(hv.x) + silu_f(a0[1]));
            v.y = pack2(bf_lo(hv.y) + silu_f(a0[2]), bf_hi(hv.y) + silu_f(a0[3]));
            v.z = pack2(bf_lo(hv.z) + silu_f(a1[0]), bf_hi(hv.z) + silu_f(a1[1]));
            v.w = pack2(bf_lo(hv.w) + silu_f(a1[2]), bf_hi(hv.w) + silu_f(a1[3]));
            *(u32x4*)(out_h + (size_t)node * D + grp * 32 + np * 8) = v;
        }
    }
}

extern "C" void kernel_launch(void* const* d_in, const int* in_sizes, int n_in,
                              void* d_out, int out_size, void* d_ws, size_t ws_size,
                              hipStream_t stream) {
    const unsigned short* h = (const unsigned short*)d_in[0];
    const unsigned short* pos = (const unsigned short*)d_in[1];
    const u32x2* e_attr = (const u32x2*)d_in[2];
    const int* src = (const int*)d_in[3];
    const int* dst = (const int*)d_in[4];
    const unsigned short* w_e = (const unsigned short*)d_in[5];
    const unsigned short* w_x = (const unsigned short*)d_in[6];
    const unsigned short* w_h = (const unsigned short*)d_in[7];
    const int N = in_sizes[1] / 3;
    const int E = in_sizes[3];

    char* ws = (char*)d_ws;
    size_t off = 0;
    int* cnt = (int*)(ws + off);            off += ((size_t)N * 4 + 255) / 256 * 256;
    u32x4* frags = (u32x4*)(ws + off);      off += (size_t)128 * 64 * 16;
    void* Bt = (void*)(ws + off);           off += (size_t)N * D * 4;
    u32x4* bins = (u32x4*)(ws + off);       off += (size_t)N * CAP * 16;

    unsigned short* out_h = (unsigned short*)d_out;
    unsigned short* out_pos = out_h + (size_t)N * D;

    constexpr bool BF16B = true;
    constexpr int T = 16, NW = 4;

    k0_prep<<<40, 256, 0, stream>>>(w_e, w_h, cnt, frags, N);
    int nbin = (E + 255) / 256;
    int ngemm = (N + 63) / 64;
    k1_bin_gemm<BF16B><<<nbin + ngemm, 256, 0, stream>>>(h, e_attr, src, dst, cnt, bins, frags, Bt, N, E, nbin);
    int nblk = (N + T - 1) / T;
    k2_agg<T, NW, BF16B><<<nblk, NW * 64, 0, stream>>>(h, pos, e_attr, src, dst, w_e, w_x, cnt, bins, frags, Bt,
                                                       out_h, out_pos, N, E);
}
```

```cpp
#include <hip/hip_runtime.h>
#include <hip/hip_bf16.h>
#include <stdint.h>

typedef __attribute__((ext_vector_type(8))) short bf16x8;
typedef __attribute__((ext_vector_type(4))) float f32x4;
typedef __attribute__((ext_vector_type(2))) float f32x2;
typedef __attribute__((ext_vector_type(4))) unsigned u32x4;
typedef __attribute__((ext_vector_type(2))) unsigned u32x2;
typedef __attribute__((ext_vector_type(2))) __bf16 bf16x2v;
typedef __attribute__((ext_vector_type(2))) unsigned short u16x2;

#define STAMP_DECL
#define STAMP(K, i)
#define STAMP_FLUSH(K)
#define WSTAMP(i)
#define WSTAMP_FLUSH()

static constexpr int D = 128;
static constexpr int T = 40;
static constexpr int MT = 3;
static constexpr int POOL = 1024;
static constexpr int CH = 2048;
static constexpr int CS = 32;
static constexpr int CC = CS - 1;
static constexpr int NT1 = 1024;
static constexpr int NT2 = 1024;

__device__ __forceinline__ float bf_lo(unsigned u) { return __uint_as_float(u << 16); }
__device__ __forceinline__ float bf_hi(unsigned u) { return __uint_as_float(u & 0xffff0000u); }
__device__ __forceinline__ float bf1(unsigned short u) { return __uint_as_float(((unsigned)u) << 16); }
__device__ __forceinline__ unsigned f2bf(float f) {
    __hip_bfloat16 b = __float2bfloat16(f);
    return (unsigned)*reinterpret_cast<unsigned short*>(&b);
}
__device__ __forceinline__ float rbf(float f) { return __uint_as_float(f2bf(f) << 16); }
__device__ __forceinline__ unsigned pack2(float lo, float hi) { return f2bf(lo) | (f2bf(hi) << 16); }

__device__ __forceinline__ unsigned cvt_pk(f32x2 v) {
    return __builtin_bit_cast(unsigned, __builtin_convertvector(v, bf16x2v));
}
__device__ __forceinline__ unsigned eq_mask16(unsigned a, unsigned b) {
    u16x2 d = __builtin_bit_cast(u16x2, a ^ b);
    u16x2 one = {1, 1};
    u16x2 m = __builtin_elementwise_min(d, one);
    m = m - one;
    return __builtin_bit_cast(unsigned, m);
}
__device__ __forceinline__ float silu_f(float x) {
    float e = __builtin_amdgcn_exp2f(x * -1.44269504088896340736f);
    return x * __builtin_amdgcn_rcpf(1.0f + e);
}

template <int CTRL> __device__ __forceinline__ float dpp_mov(float v) {
    return __builtin_bit_cast(float, __builtin_amdgcn_update_dpp(0, __builtin_bit_cast(int, v), CTRL, 0xf, 0xf, false));
}
__device__ __forceinline__ float row16_sum(float v) {
    v += dpp_mov<0x128>(v);
    v += dpp_mov<0x124>(v);
    v += dpp_mov<0x122>(v);
    v += dpp_mov<0x121>(v);
    return v;
}
__device__ __forceinline__ int wave_incl_scan(int x) {
    x += __builtin_amdgcn_update_dpp(0, x, 0x111, 0xf, 0xf, false);
    x += __builtin_amdgcn_update_dpp(0, x, 0x112, 0xf, 0xf, false);
    x += __builtin_amdgcn_update_dpp(0, x, 0x114, 0xf, 0xf, false);
    x += __builtin_amdgcn_update_dpp(0, x, 0x118, 0xf, 0xf, false);
    x += __builtin_amdgcn_update_dpp(0, x, 0x142, 0xa, 0xf, false);
    x += __builtin_amdgcn_update_dpp(0, x, 0x143, 0xc, 0xf, false);
    return x;
}

__device__ __forceinline__ int perm_ch(int nt, int i) { return (i >> 2) * 32 + nt * 4 + (i & 3); }

__global__ __launch_bounds__(NT1) void k1_kernel(const unsigned short* __restrict__ h,
                                                 const unsigned short* __restrict__ pos,
                                                 const u32x2* __restrict__ e_attr,
                                                 const int* __restrict__ src, const int* __restrict__ dst,
                                                 const unsigned short* __restrict__ w_e,
                                                 const unsigned short* __restrict__ w_h,
                                                 u32x4* __restrict__ cells, u32x4* __restrict__ frags,
                                                 unsigned short* __restrict__ Bt, u32x2* __restrict__ pos4,
                                                 int N, int E, int NB, int NC, int NG) {
    extern __shared__ __attribute__((aligned(16))) char smem[];
    const int tid = threadIdx.x;
    const int bid = blockIdx.x;
    STAMP_DECL
    STAMP(0, 0);
    if (bid < NC) {
        u32x4* sRA = (u32x4*)smem;
        int* sBk = (int*)(smem + CH * 16);
        int* lcnt = (int*)(smem + CH * 20);
        int* loff = lcnt + NB;
        const int ebase = bid * CH;
        int dd[CH / NT1], ss[CH / NT1]; u32x2 ea[CH / NT1];
#pragma unroll
        for (int it = 0; it < CH / NT1; ++it) {
            int e = ebase + it * NT1 + tid;
            int ec = e < E ? e : E - 1;
            dd[it] = dst[ec]; ss[it] = src[ec]; ea[it] = e_attr[ec];
        }
        for (int i = tid; i < NB; i += NT1) lcnt[i] = 0;
        __syncthreads();
        STAMP(0, 3);
        u32x4 ra[CH / NT1]; int bk[CH / NT1], rk[CH / NT1];
#pragma unroll
        for (int it = 0; it < CH / NT1; ++it) {
            int el = it * NT1 + tid;
            int e = ebase + el;
            bk[it] = -1;
            if (e < E) {
                int d = dd[it];
                int b = d / T;
                int dl = d - b * T;
                rk[it] = atomicAdd(&lcnt[b], 1);
                bk[it] = b;
                ra[it].x = ((unsigned)dl << 16) | (unsigned)el; ra[it].y = (unsigned)ss[it]; ra[it].z = ea[it].x; ra[it].w = ea[it].y;
            }
        }
        __syncthreads();
        STAMP(0, 4);
        if (tid < 64) {
            int carry = 0;
            for (int base = 0; base < NB; base += 64) {
                int i = base + tid;
                int v = i < NB ? lcnt[i] : 0;
                int x = wave_incl_scan(v);
                if (i < NB) loff[i] = carry + x - v;
                carry += __builtin_amdgcn_readlane(x, 63);
            }
        }
        __syncthreads();
        STAMP(0, 5);
#pragma unroll
        for (int it = 0; it < CH / NT1; ++it) {
            if (bk[it] >= 0) {
                int p = loff[bk[it]] + rk[it];
                sRA[p] = ra[it]; sBk[p] = bk[it];
            }
        }
        __syncthreads();
        STAMP(0, 1);
        const int nvalid = (E - ebase) < CH ? (E - ebase) : CH;
#pragma unroll
        for (int it = 0; it < CH / NT1; ++it) {
            int p = it * NT1 + tid;
            if (p < nvalid) {
                int b = sBk[p];
                int r = p - loff[b];
                if (r < CC) cells[((size_t)b * NC + bid) * CS + 1 + r] = sRA[p];
            }
        }
        for (int i = tid; i < NB; i += NT1) *(int*)(cells + ((size_t)i * NC + bid) * CS) = lcnt[i];
        STAMP(0, 2);
        STAMP_FLUSH(0);
        return;
    }
    const int wave = tid >> 6, l = tid & 63;
    const int lg = l & 15, grp = l >> 4;
    if (bid < NC + NG) {
        constexpr int WST = D + 8;
        unsigned short* sW = (unsigned short*)smem;
        unsigned short* sH = sW + 128 * WST;
        const int nb0 = (bid - NC) * 128;
        const u32x4* w2 = (const u32x4*)(w_e + (size_t)128 * D);
        u32x4 wv[2], hv[2];
#pragma unroll
        for (int it = 0; it < 2; ++it) {
            int ch = it * NT1 + tid;
            wv[it] = w2[ch];
            int node = nb0 + (ch >> 4);
            int nodec = node < N ? node : N - 1;
            hv[it] = *(const u32x4*)(h + (size_t)nodec * D + (ch & 15) * 8);
        }
#pragma unroll
        for (int it = 0; it < 2; ++it) {
            int ch = it * NT1 + tid;
            *(u32x4*)&sW[(ch >> 4) * WST + (ch & 15) * 8] = wv[it];
            *(u32x4*)&sH[(ch >> 4) * WST + (ch & 15) * 8] = hv[it];
        }
        __syncthreads();
        STAMP(0, 1);
        const int np = wave & 3, mg = wave >> 2;
        bf16x8 hb[2][4];
#pragma unroll
        for (int m = 0; m < 2; ++m)
#pragma unroll
            for (int ks = 0; ks < 4; ++ks) hb[m][ks] = *(const bf16x8*)&sH[(mg * 32 + m * 16 + lg) * WST + ks * 32 + grp * 8];
        f32x4 acc[2][2];
#pragma unroll
        for (int m = 0; m < 2; ++m) { acc[m][0] = (f32x4){0.f, 0.f, 0.f, 0.f}; acc[m][1] = (f32x4){0.f, 0.f, 0.f, 0.f}; }
#pragma unroll
        for (int q = 0; q < 2; ++q) {
            int c = perm_ch(np * 2 + q, lg);
#pragma unroll
            for (int ks = 0; ks < 4; ++ks) {
                bf16x8 wf;
                int k0 = ks * 32 + grp * 8;
#pragma unroll
                for (int j = 0; j < 8; ++j) wf[j] = (short)sW[(k0 + j) * WST + c];
                acc[0][q] = __builtin_amdgcn_mfma_f32_16x16x32_bf16(wf, hb[0][ks], acc[0][q], 0, 0, 0);
                acc[1][q] = __builtin_amdgcn_mfma_f32_16x16x32_bf16(wf, hb[1][ks], acc[1][q], 0, 0, 0);
            }
        }
#pragma unroll
        for (int m = 0; m < 2; ++m) {
            int node = nb0 + mg * 32 + m * 16 + lg;
            if (node < N) {
                u32x4 v;
                v.x = pack2(acc[m][0][0], acc[m][0][1]); v.y = pack2(acc[m][0][2], acc[m][0][3]);
                v.z = pack2(acc[m][1][0], acc[m][1][1]); v.w = pack2(acc[m][1][2], acc[m][1][3]);
                *(u32x4*)(Bt + (size_t)node * D + grp * 32 + np * 8) = v;
            }
        }
        STAMP(0, 2);
        STAMP_FLUSH(0);
        return;
    }
    {
        int t = (bid - NC - NG) * NT1 + tid;
        if (t < 96 * 64) {
            int f = t >> 6;
            const unsigned short* W;
            int nt, ks;
            if (f < 32) { W = w_e; nt = f >> 2; ks = f & 3; }
            else { int g = f - 32; W = w_h; nt = g >> 3; ks = g & 7; }
            int c = perm_ch(nt, lg);
            int k0 = ks * 32 + 8 * grp;
            unsigned v[8];
#pragma unroll
            for (int j = 0; j < 8; ++j) v[j] = W[(size_t)(k0 + j) * D + c];
            u32x4 o;
            o.x = v[0] | (v[1] << 16); o.y = v[2] | (v[3] << 16); o.z = v[4] | (v[5] << 16); o.w = v[6] | (v[7] << 16);
            frags[t] = o;
        } else {
            for (int n = t - 96 * 64; n < N; n += (gridDim.x - NC - NG) * NT1 - 96 * 64) {
                u32x2 o;
                o.x = (unsigned)pos[n * 3] | ((unsigned)pos[n * 3 + 1] << 16);
                o.y = (unsigned)pos[n * 3 + 2];
                pos4[n] = o;
            }
        }
    }
}

static constexpr int MST = D + 8;
static constexpr int PST = D + 16;
static constexpr int NU_MAX = POOL / 32;
static constexpr int NPR = NU_MAX + T;

__global__ __launch_bounds__(NT2) void k2_kernel(const unsigned short* __restrict__ h,
                                                 const unsigned short* __restrict__ pos,
                                                 const u32x2* __restrict__ e_attr,
                                                 const int* __restrict__ src, const int* __restrict__ dst,
                                                 const unsigned short* __restrict__ w_e,
                                                 const unsigned short* __restrict__ w_x,
                                                 const u32x4* __restrict__ cells,
                                                 const u32x4* __restrict__ frags, const unsigned short* __restrict__ Bt,
                                                 const u32x2* __restrict__ pos4,
                                                 unsigned short* __restrict__ out_h, unsigned short* __restrict__ out_pos,
                                                 int N, int E, int NC) {
    __shared__ __attribute__((aligned(16))) u32x4 sWh[64 * 64];
    __shared__ __attribute__((aligned(16))) float sProw[NPR * PST];
    __shared__ __attribute__((aligned(16))) float sPpos[NPR * 4];
    __shared__ __attribute__((aligned(16))) unsigned short sH[MT * 16 * MST];
    __shared__ __attribute__((aligned(16))) unsigned short sA[MT * 16 * MST - 8 * MST];
    __shared__ __attribute__((aligned(16))) u32x4 sRec[POOL];
    __shared__ __attribute__((aligned(16))) unsigned short sCf[3][POOL];
    __shared__ __attribute__((aligned(16))) unsigned short sDl[POOL + 32];
    __shared__ float sWx[D];
    __shared__ float sPosT[T * 3 + 8];
    __shared__ int sCnt[64], sOff[64];
    __shared__ int sUf[64], sUl[64];
    __shared__ int sMisc[4];
    unsigned* sKey = (unsigned*)sProw;

    const int tid = threadIdx.x;
    const int wave = __builtin_amdgcn_readfirstlane(tid >> 6);
    const int l = tid & 63;
    const int lg = l & 15, grp = l >> 4;
    const int b = blockIdx.x;
    const int n0 = b * T;
    STAMP_DECL
    STAMP(1, 0);

    const u32x4* cb = cells + (size_t)b * NC * CS;
    constexpr int MAXR = 3;
    u32x4 ra[MAXR];
#pragma unroll
    for (int it = 0; it < MAXR; ++it) {
        int s = it * NT2 + tid;
        int sc = s < NC * CS ? s : NC * CS - 1;
        ra[it] = cb[sc];
    }
    u32x4 whv[4];
#pragma unroll
    for (int i = 0; i < 4; ++i) whv[i] = frags[32 * 64 + i * NT2 + tid];
    u32x4 hv = {0u, 0u, 0u, 0u};
    if (tid < MT * 16 * 16) {
        int node = n0 + (tid >> 4);
        int nodec = node < N ? node : N - 1;
        hv = *(const u32x4*)(h + (size_t)nodec * D + (tid & 15) * 8);
    }
    float posv = 0.f;
    if (tid < T * 3) posv = (n0 * 3 + tid < N * 3) ? bf1(pos[n0 * 3 + tid]) : 0.f;
    float wxv = 0.f;
    if (tid < D) wxv = bf1(w_x[tid]);
    u32x4 w1f[8];
    if (wave < 4) {
#pragma unroll
        for (int i = 0; i < 8; ++i) w1f[i] = frags[(wave * 8 + i) * 64 + l];
    }
    unsigned wf[2][3];
#pragma unroll
    for (int a = 0; a < 2; ++a) {
        const unsigned short* wp = w_e + (size_t)256 * D + (4 * a + grp) * 16 + lg;
        unsigned v0 = wp[0], v1 = wp[D], v2 = wp[2 * D], v3 = wp[3 * D], v4 = wp[4 * D];
        wf[a][0] = v0 | (v1 << 16); wf[a][1] = v2 | (v3 << 16); wf[a][2] = v4;
    }
    if (tid < 64) sCnt[tid] = 0;
    if (tid < 4) sMisc[tid] = 0;
    __syncthreads();
    STAMP(1, 6);

    int rank[MAXR];
    unsigned key[MAXR];
    u32x2 ps[MAXR];
    unsigned pfa[MAXR], pfb[MAXR];
#pragma unroll
    for (int it = 0; it < MAXR; ++it) {
        int s = it * NT2 + tid;
        int c = s >> 5, r = s & 31;
        int cc = __builtin_amdgcn_ds_bpermute((l & 32) << 2, (int)ra[it].x);
        rank[it] = -1;
        key[it] = 0;
        ps[it] = (u32x2){0u, 0u};
        if (c < NC) {
            if (cc > CC || cc < 0) { if (r == 0) sMisc[0] = 1; }
            else if (r >= 1 && r <= cc) {
                int dl = ra[it].x >> 16;
                rank[it] = atomicAdd(&sCnt[dl], 1) | (dl << 16);
                key[it] = (unsigned)(c * CH) + (ra[it].x & 0xffffu);
                ps[it] = pos4[ra[it].y];
            }
        }
    }
#pragma unroll
    for (int it = 0; it < MAXR; ++it) {
        pfa[it] = 0u; pfb[it] = 0u;
        if (rank[it] >= 0) {
            const unsigned* brow = (const unsigned*)(Bt + (size_t)ra[it].y * D);
            pfa[it] = brow[0]; pfb[it] = brow[32];
        }
    }
    if (NC * CS > MAXR * NT2 && tid == 0) sMisc[0] = 1;
    STAMP(1, 7);
    if (tid < MT * 16 * 16) *(u32x4*)&sH[(tid >> 4) * MST + (tid & 15) * 8] = hv;
#pragma unroll
    for (int i = 0; i < 4; ++i) sWh[i * NT2 + tid] = whv[i];
    if (tid < T * 3) sPosT[tid] = posv;
    if (tid < D) sWx[tid] = wxv;
    __syncthreads();
    STAMP(1, 5);

    if (wave < 4) {
#pragma unroll
        for (int mt = 0; mt < MT; ++mt) {
            f32x4 a0 = {0.f, 0.f, 0.f, 0.f}, a1 = {0.f, 0.f, 0.f, 0.f};
#pragma unroll
            for (int ks = 0; ks < 4; ++ks) {
                bf16x8 hb = *(const bf16x8*)&sH[(mt * 16 + lg) * MST + ks * 32 + grp * 8];
                a0 = __builtin_amdgcn_mfma_f32_16x16x32_bf16(__builtin_bit_cast(bf16x8, w1f[ks]), hb, a0, 0, 0, 0);
                a1 = __builtin_amdgcn_mfma_f32_16x16x32_bf16(__builtin_bit_cast(bf16x8, w1f[4 + ks]), hb, a1, 0, 0, 0);
            }
            if (mt * 16 + lg < T) {
                u32x4 v;
                v.x = pack2(a0[0], a0[1]); v.y = pack2(a0[2], a0[3]); v.z = pack2(a1[0], a1[1]); v.w = pack2(a1[2], a1[3]);
                *(u32x4*)&sA[(mt * 16 + lg) * MST + grp * 32 + wave * 8] = v;
            }
        }
    }
    unsigned sel[2][4];
#pragma unroll
    for (int hf = 0; hf < 2; ++hf)
#pragma unroll
        for (int w = 0; w < 4; ++w) {
            int k0 = grp * 8 + w * 2, tk = hf * 16 + lg;
            sel[hf][w] = (k0 == tk ? 0x3f80u : 0u) | (k0 + 1 == tk ? 0x3f800000u : 0u);
        }

    int round_pos = 0;
    float* macc = (float*)sWh;
    u32x4* sRaw = (u32x4*)(sKey + POOL);
    u32x2* sRawP = (u32x2*)(sKey + POOL + POOL * 4);
    int total;
    bool slow;
    {
        int cntl = sCnt[l];
        int incl = wave_incl_scan(cntl);
        int excl = incl - cntl;
        total = __builtin_amdgcn_readlane(incl, 63);
        slow = sMisc[0] != 0 || total > POOL;
        if (!slow) {
#pragma unroll
            for (int it = 0; it < MAXR; ++it) {
                int dl = rank[it] >> 16;
                int o = __builtin_amdgcn_ds_bpermute((dl & 63) << 2, excl);
                if (rank[it] >= 0) {
                    int p = o + (rank[it] & 0xffff);
                    sKey[p] = key[it]; sRaw[p] = ra[it]; sRawP[p] = ps[it];
                }
            }
            if (wave == 0) sOff[l] = excl;
        }
    }
    if (slow) {
        __syncthreads();
        for (int i = tid; i < T * PST; i += NT2) macc[i] = 0.f;
    }
    for (;;) {
        int tid = threadIdx.x;
        asm volatile("" : "+v"(tid));
        const int l = tid & 63;
        const int lg = l & 15, grp = l >> 4;
        if (slow) {
            __syncthreads();
            if (tid < 64) sCnt[tid] = 0;
            if (tid == 0) sMisc[1] = 0;
            __syncthreads();
            int e = round_pos + tid;
            round_pos += NT2;
            int rk = -1, dl = 0;
            u32x4 rr = {0u, 0u, 0u, 0u};
            u32x2 pp = {0u, 0u};
            if (e < E) {
                dl = dst[e] - n0;
                if (dl >= 0 && dl < T) {
                    int s = src[e];
                    u32x2 ea = e_attr[e];
                    rr.x = (unsigned)dl << 16; rr.y = (unsigned)s; rr.z = ea.x; rr.w = ea.y;
                    pp = pos4[s];
                    rk = atomicAdd(&sCnt[dl], 1);
                }
            }
            __syncthreads();
            int cntl = sCnt[l];
            int incl = wave_incl_scan(cntl);
            int excl = incl - cntl;
            total = __builtin_amdgcn_readlane(incl, 63);
            int o = __builtin_amdgcn_ds_bpermute((dl & 63) << 2, excl);
            if (rk >= 0) { int p = o + rk; sKey[p] = (unsigned)e; sRaw[p] = rr; sRawP[p] = pp; }
            if (wave == 0) sOff[l] = excl;
        }
        __syncthreads();
        if (tid < total) {
            u32x4 rr = sRaw[tid];
            u32x2 pp = sRawP[tid];
            unsigned ky = sKey[tid];
            int dl = rr.x >> 16;
            int o = sOff[dl], cnt = sCnt[dl];
            int r = 0;
            for (int j = 0; j < cnt; ++j) r += sKey[o + j] < ky ? 1 : 0;
            int p = o + r;
            float d0 = rbf(sPosT[dl * 3 + 0] - bf_lo(pp.x));
            float d1 = rbf(sPosT[dl * 3 + 1] - bf_hi(pp.x));
            float d2 = rbf(sPosT[dl * 3 + 2] - bf_lo(pp.y));
            float q = rbf(rbf(d0 * d0) + rbf(d1 * d1) + rbf(d2 * d2));
            u32x4 rec;
            rec.x = (__float_as_uint(q) >> 16) | (rr.z << 16);
            rec.y = (rr.z >> 16) | (rr.w << 16);
            rec.z = (rr.w >> 16) | ((unsigned)dl << 16);
            rec.w = rr.y;
            sRec[p] = rec;
            sCf[0][p] = (unsigned short)(__float_as_uint(d0) >> 16);
            sCf[1][p] = (unsigned short)(__float_as_uint(d1) >> 16);
            sCf[2][p] = (unsigned short)(__float_as_uint(d2) >> 16);
            sDl[p] = (unsigned short)dl;
        }
        if (tid < 32) sDl[total + tid] = 0xffffu;
        if (tid >= 64 && tid < 64 + T) {
            int n = tid - 64;
            int o = sOff[n], cnt = sCnt[n];
            sUf[n] = o >> 5;
            sUl[n] = cnt > 0 ? (o + cnt - 1) >> 5 : -1;
        }
        __syncthreads();
#pragma unroll
        for (int it = 0; it < MAXR; ++it) { asm volatile("" :: "v"(pfa[it]), "v"(pfb[it])); pfa[it] = 0u; pfb[it] = 0u; }
        STAMP(1, 1);

        const int NU = (total + 31) >> 5;
        int wsi = 0;
        WSTAMP(wsi); wsi++;
        int un = 0;
        if (l == 0) un = atomicAdd(&sMisc[1], 1);
        un = __builtin_amdgcn_readfirstlane(un);
        u32x4 recn = {0u, 0u, 0u, 0u};
        u32x4 Xn[4];
#pragma unroll
        for (int kc = 0; kc < 4; ++kc) Xn[kc] = (u32x4){0u, 0u, 0u, 0u};
        if (un < NU) {
            int e = un * 32 + lg;
            recn = sRec[e < total ? e : total - 1];
            const unsigned short* brow = Bt + (size_t)recn.w * D + grp * 8;
#pragma unroll
            for (int kc = 0; kc < 4; ++kc) Xn[kc] = *(const u32x4*)(brow + kc * 32);
        }
        while (un < NU) {
            const int u = un;
            const int p0 = u * 32;
            unsigned S[2][8][2];
            u32x4 rec0 = recn, X0[4], rec1, X1[4];
#pragma unroll
            for (int kc = 0; kc < 4; ++kc) X0[kc] = Xn[kc];
            {
                int e = p0 + 16 + lg;
                rec1 = sRec[e < total ? e : total - 1];
                const unsigned short* brow = Bt + (size_t)rec1.w * D + grp * 8;
#pragma unroll
                for (int kc = 0; kc < 4; ++kc) X1[kc] = *(const u32x4*)(brow + kc * 32);
            }
            if (l == 0) un = atomicAdd(&sMisc[1], 1);
            un = __builtin_amdgcn_readfirstlane(un);
#pragma unroll
            for (int bt = 0; bt < 2; ++bt) {
                const u32x4 rec = bt == 0 ? rec0 : rec1;
                const unsigned short* arow = &sA[(rec.z >> 16) * MST + grp * 8];
                unsigned f2 = rec.z & 0xffffu;
#pragma unroll
                for (int hg = 0; hg < 2; ++hg) {
                    f32x4 acc[4];
#pragma unroll
                    for (int g = 0; g < 4; ++g) {
                        int nt = hg * 4 + g;
                        u32x4 F;
                        F.x = grp == g ? rec.x : 0u; F.y = grp == g ? rec.y : 0u; F.z = grp == g ? f2 : 0u; F.w = 0u;
                        u32x4 Wf = {wf[hg][0], wf[hg][1], wf[hg][2], 0u};
                        u32x4 Y = *(const u32x4*)(arow + (nt >> 1) * 32);
                        u32x4 Sl = {sel[nt & 1][0], sel[nt & 1][1], sel[nt & 1][2], sel[nt & 1][3]};
                        u32x4 Xv = bt == 0 ? X0[nt >> 1] : X1[nt >> 1];
                        f32x4 a = {0.f, 0.f, 0.f, 0.f};
                        a = __builtin_amdgcn_mfma_f32_16x16x32_bf16(__builtin_bit_cast(bf16x8, F), __builtin_bit_cast(bf16x8, Wf), a, 0, 0, 0);
                        a = __builtin_amdgcn_mfma_f32_16x16x32_bf16(__builtin_bit_cast(bf16x8, Y), __builtin_bit_cast(bf16x8, Sl), a, 0, 0, 0);
                        a = __builtin_amdgcn_mfma_f32_16x16x32_bf16(__builtin_bit_cast(bf16x8, Xv), __builtin_bit_cast(bf16x8, Sl), a, 0, 0, 0);
                        acc[g] = a;
                    }
                    if (bt == 1 && hg == 0) {
                        __builtin_amdgcn_sched_barrier(0);
                        if (un < NU) {
                            int e = un * 32 + lg;
                            recn = sRec[e < total ? e : total - 1];
                            const unsigned short* brow = Bt + (size_t)recn.w * D + grp * 8;
#pragma unroll
                            for (int kc = 0; kc < 4; ++kc) Xn[kc] = *(const u32x4*)(brow + kc * 32);
                        }
                        __builtin_amdgcn_sched_barrier(0);
                    }
#pragma unroll
                    for (int g = 0; g < 4; ++g) {
#pragma unroll
                        for (int hh = 0; hh < 2; ++hh) {
                            f32x2 x = {acc[g][hh * 2], acc[g][hh * 2 + 1]};
                            f32x2 tt = x * -1.44269504088896340736f;
                            f32x2 ex = {__builtin_amdgcn_exp2f(tt[0]), __builtin_amdgcn_exp2f(tt[1])};
                            ex += 1.0f;
                            f32x2 rc = {__builtin_amdgcn_rcpf(ex[0]), __builtin_amdgcn_rcpf(ex[1])};
                            S[bt][hg * 4 + g][hh] = cvt_pk(x * rc);
                        }
                    }
                    __builtin_amdgcn_sched_barrier(0);
                }
            }
            WSTAMP(wsi); wsi++;
            {
                int l = threadIdx.x;
                asm volatile("" : "+v"(l));
                l &= 63;
                const int lg = l & 15, grp = l >> 4;
                int lastp = p0 + 31 < total ? p0 + 31 : total - 1;
                int nlo = __builtin_amdgcn_readfirstlane((int)sDl[p0]);
                int nhi = __builtin_amdgcn_readfirstlane((int)sDl[lastp]);
                u32x2 dv0 = *(const u32x2*)&sDl[p0 + 4 * grp];
                u32x2 dv1 = *(const u32x2*)&sDl[p0 + 16 + 4 * grp];
                int c = lg & 3;
                int ci = c > 0 ? c - 1 : 0;
                u32x2 cf0 = *(const u32x2*)&sCf[ci][p0 + 4 * grp];
                u32x2 cf1 = *(const u32x2*)&sCf[ci][p0 + 16 + 4 * grp];
                if (c == 0) { cf0 = (u32x2){0x3f803f80u, 0x3f803f80u}; cf1 = cf0; }
                for (int base = nlo; base <= nhi; base += 4) {
                    unsigned tg = (unsigned)(base + (lg >> 2)) * 0x10001u;
                    u32x4 G;
                    G.x = eq_mask16(dv0.x, tg) & cf0.x; G.y = eq_mask16(dv0.y, tg) & cf0.y;
                    G.z = eq_mask16(dv1.x, tg) & cf1.x; G.w = eq_mask16(dv1.y, tg) & cf1.y;
                    int node = base + grp;
                    bool act = node <= nhi;
                    float* prow = &sProw[(u + node) * PST + lg];
                    float q1 = 0.f, q2 = 0.f, q3 = 0.f;
#pragma unroll
                    for (int nt = 0; nt < 8; ++nt) {
                        u32x4 Sv = {S[0][nt][0], S[0][nt][1], S[1][nt][0], S[1][nt][1]};
                        f32x4 M = {0.f, 0.f, 0.f, 0.f};
                        M = __builtin_amdgcn_mfma_f32_16x16x32_bf16(__builtin_bit_cast(bf16x8, G), __builtin_bit_cast(bf16x8, Sv), M, 0, 0, 0);
                        float w = sWx[nt * 16 + lg];
                        if (act) prow[nt * 16] = M[0];
                        q1 = fmaf(M[1], w, q1); q2 = fmaf(M[2], w, q2); q3 = fmaf(M[3], w, q3);
                    }
                    q1 = row16_sum(q1); q2 = row16_sum(q2); q3 = row16_sum(q3);
                    if (act && lg < 3) sPpos[(u + node) * 4 + lg] = lg == 0 ? q1 : (lg == 1 ? q2 : q3);
                }
            }
            WSTAMP(wsi); wsi++;
        }
        STAMP(1, 2);
        __syncthreads();
        STAMP(1, 3);
        if (!slow) break;
        tid = threadIdx.x;
        asm volatile("" : "+v"(tid));
        for (int i = tid; i < T * 32; i += NT2) {
            int n = i >> 5, c4 = i & 31;
            f32x4 a = *(const f32x4*)&macc[n * PST + c4 * 4];
            for (int uu = sUf[n]; uu <= sUl[n]; ++uu) a += *(const f32x4*)&sProw[(uu + n) * PST + c4 * 4];
            *(f32x4*)&macc[n * PST + c4 * 4] = a;
        }
        if (tid < T * 3) {
            int n = tid / 3, c = tid - n * 3;
            float a = macc[n * PST + D + c];
            for (int uu = sUf[n]; uu <= sUl[n]; ++uu) a += sPpos[(uu + n) * 4 + c];
            macc[n * PST + D + c] = a;
        }
        if (round_pos >= E) break;
    }
    if (slow) {
        int tid = threadIdx.x;
        asm volatile("" : "+v"(tid));
        __syncthreads();
        for (int i = tid; i < T * 32; i += NT2) {
            int n = i >> 5, c4 = i & 31;
            *(f32x4*)&sProw[n * PST + c4 * 4] = *(const f32x4*)&macc[n * PST + c4 * 4];
        }
        if (tid < T * 3) { int n = tid / 3, c = tid - n * 3; sPpos[n * 4 + c] = macc[n * PST + D + c]; }
        if (tid < T) { sUf[tid] = 0; sUl[tid] = 0; }
        __syncthreads();
#pragma unroll
        for (int i = 0; i < 4; ++i) sWh[i * NT2 + tid] = frags[32 * 64 + i * NT2 + tid];
        __syncthreads();
    }

    if (wave < MT * 4) {
        int l = threadIdx.x;
        asm volatile("" : "+v"(l));
        l &= 63;
        const int lg = l & 15, grp = l >> 4;
        int mt = wave >> 2, np = wave & 3;
        int row = mt * 16 + lg;
        int node = n0 + row;
        bool valid = (row < T) && (node < N);
        int uf = valid ? sUf[row] : 0, ul = valid ? sUl[row] : -1;
        f32x4 a0 = {0.f, 0.f, 0.f, 0.f}, a1 = {0.f, 0.f, 0.f, 0.f};
#pragma unroll
        for (int ks = 0; ks < 8; ++ks) {
            bf16x8 xb;
            if (ks < 4) xb = *(const bf16x8*)&sH[row * MST + ks * 32 + grp * 8];
            else {
                f32x4 m0 = {0.f, 0.f, 0.f, 0.f}, m1 = {0.f, 0.f, 0.f, 0.f};
                for (int uu = uf; uu <= ul; ++uu) {
                    const float* pr = &sProw[(uu + row) * PST + (ks - 4) * 32 + grp * 8];
                    m0 += *(const f32x4*)pr; m1 += *(const f32x4*)(pr + 4);
                }
                u32x4 v;
                v.x = cvt_pk((f32x2){m0[0], m0[1]}); v.y = cvt_pk((f32x2){m0[2], m0[3]});
                v.z = cvt_pk((f32x2){m1[0], m1[1]}); v.w = cvt_pk((f32x2){m1[2], m1[3]});
                xb = __builtin_bit_cast(bf16x8, v);
            }
            a0 = __builtin_amdgcn_mfma_f32_16x16x32_bf16(__builtin_bit_cast(bf16x8, sWh[((np * 2) * 8 + ks) * 64 + l]), xb, a0, 0, 0, 0);
            a1 = __builtin_amdgcn_mfma_f32_16x16x32_bf16(__builtin_bit_cast(bf16x8, sWh[((np * 2 + 1) * 8 + ks) * 64 + l]), xb, a1, 0, 0, 0);
        }
        if (valid) {
            u32x4 hv2 = *(const u32x4*)&sH[row * MST + grp * 32 + np * 8];
            u32x4 v;
            v.x = pack2(bf_lo(hv2.x) + silu_f(a0[0]), bf_hi(hv2.x) + silu_f(a0[1]));
            v.y = pack2(bf_lo(hv2.y) + silu_f(a0[2]), bf_hi(hv2.y) + silu_f(a0[3]));
            v.z = pack2(bf_lo(hv2.z) + silu_f(a1[0]), bf_hi(hv2.z) + silu_f(a1[1]));
            v.w = pack2(bf_lo(hv2.w) + silu_f(a1[2]), bf_hi(hv2.w) + silu_f(a1[3]));
            *(u32x4*)(out_h + (size_t)node * D + grp * 32 + np * 8) = v;
        }
    } else {
        int t = threadIdx.x;
        asm volatile("" : "+v"(t));
        t -= MT * 4 * 64;
        if (t < T * 3 && n0 * 3 + t < N * 3) {
            int n = t / 3, c = t - n * 3;
            float a = 0.f;
            for (int uu = sUf[n]; uu <= sUl[n]; ++uu) a += sPpos[(uu + n) * 4 + c];
            out_pos[(size_t)n0 * 3 + t] = (unsigned short)f2bf(sPosT[t] + a);
        }
    }
    STAMP(1, 4);
    STAMP_FLUSH(1);
    WSTAMP_FLUSH();
}

extern "C" void kernel_launch(void* const* d_in, const int* in_sizes, int n_in,
                              void* d_out, int out_size, void* d_ws, size_t ws_size,
                              hipStream_t stream) {
    const unsigned short* h = (const unsigned short*)d_in[0];
    const unsigned short* pos = (const unsigned short*)d_in[1];
    const u32x2* e_attr = (const u32x2*)d_in[2];
    const int* src = (const int*)d_in[3];
    const int* dst = (const int*)d_in[4];
    const unsigned short* w_e = (const unsigned short*)d_in[5];
    const unsigned short* w_x = (const unsigned short*)d_in[6];
    const unsigned short* w_h = (const unsigned short*)d_in[7];
    const int N = in_sizes[1] / 3;
    const int E = in_sizes[3];
    const int NB = (N + T - 1) / T;
    const int NC = (E + CH - 1) / CH;
    const int NG = (N + 127) / 128;

    char* ws = (char*)d_ws;
    size_t off = 0;
    auto take = [&](size_t bytes) { char* p = ws + off; off += (bytes + 255) / 256 * 256; return p; };
    u32x4* frags = (u32x4*)take((size_t)96 * 64 * 16);
    unsigned short* Bt = (unsigned short*)take((size_t)N * D * 2);
    u32x2* pos4 = (u32x2*)take((size_t)N * 8);
    u32x4* cells = (u32x4*)take((size_t)NB * NC * CS * 16);

    unsigned short* out_h = (unsigned short*)d_out;
    unsigned short* out_pos = out_h + (size_t)N * D;

    size_t lds1 = (size_t)CH * 20 + (size_t)NB * 8;
    if (lds1 < (size_t)2 * 128 * (D + 8) * 2) lds1 = (size_t)2 * 128 * (D + 8) * 2;
    (void)hipFuncSetAttribute((const void*)k1_kernel, hipFuncAttributeMaxDynamicSharedMemorySize, (int)lds1);
    k1_kernel<<<NC + NG + 16, NT1, lds1, stream>>>(h, pos, e_attr, src, dst, w_e, w_h, cells, frags, Bt, pos4,
                                                    N, E, NB, NC, NG);
    k2_kernel<<<NB, NT2, 0, stream>>>(h, pos, e_attr, src, dst, w_e, w_x, cells, frags, Bt, pos4,
                                      out_h, out_pos, N, E, NC);
}
```

```cpp
#include <hip/hip_runtime.h>
#include <hip/hip_bf16.h>
#include <stdint.h>

typedef __attribute__((ext_vector_type(8))) short bf16x8;
typedef __attribute__((ext_vector_type(4))) float f32x4;
typedef __attribute__((ext_vector_type(2))) float f32x2;
typedef __attribute__((ext_vector_type(4))) unsigned u32x4;
typedef __attribute__((ext_vector_type(2))) unsigned u32x2;
typedef __attribute__((ext_vector_type(2))) __bf16 bf16x2v;
typedef __attribute__((ext_vector_type(2))) unsigned short u16x2;

#define STAMP_DECL
#define STAMP(K, i)
#define STAMP_FLUSH(K)
#define WSTAMP(i)
#define WSTAMP_FLUSH()

static constexpr int D = 128;
static constexpr int T = 40;
static constexpr int MT = 3;
static constexpr int POOL = 1024;
static constexpr int CH = 2048;
static constexpr int CS = 32;
static constexpr int CC = CS - 1;
static constexpr int NT1 = 1024;
static constexpr int NT2 = 1024;

__device__ __forceinline__ float bf_lo(unsigned u) { return __uint_as_float(u << 16); }
__device__ __forceinline__ float bf_hi(unsigned u) { return __uint_as_float(u & 0xffff0000u); }
__device__ __forceinline__ float bf1(unsigned short u) { return __uint_as_float(((unsigned)u) << 16); }
__device__ __forceinline__ unsigned f2bf(float f) {
    __hip_bfloat16 b = __float2bfloat16(f);
    return (unsigned)*reinterpret_cast<unsigned short*>(&b);
}
__device__ __forceinline__ float rbf(float f) { return __uint_as_float(f2bf(f) << 16); }
__device__ __forceinline__ unsigned pack2(float lo, float hi) { return f2bf(lo) | (f2bf(hi) << 16); }

__device__ __forceinline__ unsigned cvt_pk(f32x2 v) {
    return __builtin_bit_cast(unsigned, __builtin_convertvector(v, bf16x2v));
}
__device__ __forceinline__ unsigned eq_mask16(unsigned a, unsigned b) {
    u16x2 d = __builtin_bit_cast(u16x2, a ^ b);
    u16x2 one = {1, 1};
    u16x2 m = __builtin_elementwise_min(d, one);
    m = m - one;
    return __builtin_bit_cast(unsigned, m);
}
__device__ __forceinline__ void lds_barrier() {
    asm volatile("s_waitcnt lgkmcnt(0)" ::: "memory");
    __builtin_amdgcn_s_barrier();
    asm volatile("" ::: "memory");
}
__device__ __forceinline__ float silu_f(float x) {
    float e = __builtin_amdgcn_exp2f(x * -1.44269504088896340736f);
    return x * __builtin_amdgcn_rcpf(1.0f + e);
}

template <int CTRL> __device__ __forceinline__ float dpp_mov(float v) {
    return __builtin_bit_cast(float, __builtin_amdgcn_update_dpp(0, __builtin_bit_cast(int, v), CTRL, 0xf, 0xf, false));
}
__device__ __forceinline__ float row16_sum(float v) {
    v += dpp_mov<0x128>(v);
    v += dpp_mov<0x124>(v);
    v += dpp_mov<0x122>(v);
    v += dpp_mov<0x121>(v);
    return v;
}
__device__ __forceinline__ int wave_incl_scan(int x) {
    x += __builtin_amdgcn_update_dpp(0, x, 0x111, 0xf, 0xf, false);
    x += __builtin_amdgcn_update_dpp(0, x, 0x112, 0xf, 0xf, false);
    x += __builtin_amdgcn_update_dpp(0, x, 0x114, 0xf, 0xf, false);
    x += __builtin_amdgcn_update_dpp(0, x, 0x118, 0xf, 0xf, false);
    x += __builtin_amdgcn_update_dpp(0, x, 0x142, 0xa, 0xf, false);
    x += __builtin_amdgcn_update_dpp(0, x, 0x143, 0xc, 0xf, false);
    return x;
}

__device__ __forceinline__ int perm_ch(int nt, int i) { return (i >> 2) * 32 + nt * 4 + (i & 3); }

__global__ __launch_bounds__(NT1) void k1_kernel(const unsigned short* __restrict__ h,
                                                 const unsigned short* __restrict__ pos,
                                                 const u32x2* __restrict__ e_attr,
                                                 const int* __restrict__ src, const int* __restrict__ dst,
                                                 const unsigned short* __restrict__ w_e,
                                                 const unsigned short* __restrict__ w_h,
                                                 u32x4* __restrict__ cells, u32x4* __restrict__ frags,
                                                 unsigned short* __restrict__ Bt, u32x2* __restrict__ pos4,
                                                 int N, int E, int NB, int NC, int NG) {
    extern __shared__ __attribute__((aligned(16))) char smem[];
    const int tid = threadIdx.x;
    const int bid = blockIdx.x;
    STAMP_DECL
    STAMP(0, 0);
    if (bid < NC) {
        u32x4* sRA = (u32x4*)smem;
        int* sBk = (int*)(smem + CH * 16);
        int* lcnt = (int*)(smem + CH * 20);
        int* loff = lcnt + NB;
        const int ebase = bid * CH;
        int dd[CH / NT1], ss[CH / NT1]; u32x2 ea[CH / NT1];
#pragma unroll
        for (int it = 0; it < CH / NT1; ++it) {
            int e = ebase + it * NT1 + tid;
            int ec = e < E ? e : E - 1;
            dd[it] = dst[ec]; ss[it] = src[ec]; ea[it] = e_attr[ec];
        }
        for (int i = tid; i < NB; i += NT1) lcnt[i] = 0;
        __syncthreads();
        STAMP(0, 3);
        u32x4 ra[CH / NT1]; int bk[CH / NT1], rk[CH / NT1];
#pragma unroll
        for (int it = 0; it < CH / NT1; ++it) {
            int el = it * NT1 + tid;
            int e = ebase + el;
            bk[it] = -1;
            if (e < E) {
                int d = dd[it];
                int b = d / T;
                int dl = d - b * T;
                rk[it] = atomicAdd(&lcnt[b], 1);
                bk[it] = b;
                ra[it].x = ((unsigned)dl << 16) | (unsigned)el; ra[it].y = (unsigned)ss[it]; ra[it].z = ea[it].x; ra[it].w = ea[it].y;
            }
        }
        __syncthreads();
        STAMP(0, 4);
        if (tid < 64) {
            int carry = 0;
            for (int base = 0; base < NB; base += 64) {
                int i = base + tid;
                int v = i < NB ? lcnt[i] : 0;
                int x = wave_incl_scan(v);
                if (i < NB) loff[i] = carry + x - v;
                carry += __builtin_amdgcn_readlane(x, 63);
            }
        }
        __syncthreads();
        STAMP(0, 5);
#pragma unroll
        for (int it = 0; it < CH / NT1; ++it) {
            if (bk[it] >= 0) {
                int p = loff[bk[it]] + rk[it];
                sRA[p] = ra[it]; sBk[p] = bk[it];
            }
        }
        __syncthreads();
        STAMP(0, 1);
        const int nvalid = (E - ebase) < CH ? (E - ebase) : CH;
#pragma unroll
        for (int it = 0; it < CH / NT1; ++it) {
            int p = it * NT1 + tid;
            if (p < nvalid) {
                int b = sBk[p];
                int r = p - loff[b];
                if (r < CC) cells[((size_t)b * NC + bid) * CS + 1 + r] = sRA[p];
            }
        }
        for (int i = tid; i < NB; i += NT1) *(int*)(cells + ((size_t)i * NC + bid) * CS) = lcnt[i];
        STAMP(0, 2);
        STAMP_FLUSH(0);
        return;
    }
    const int wave = tid >> 6, l = tid & 63;
    const int lg = l & 15, grp = l >> 4;
    if (bid < NC + NG) {
        constexpr int WST = D + 8;
        unsigned short* sW = (unsigned short*)smem;
        unsigned short* sH = sW + 128 * WST;
        const int nb0 = (bid - NC) * 128;
        const u32x4* w2 = (const u32x4*)(w_e + (size_t)128 * D);
        u32x4 wv[2], hv[2];
#pragma unroll
        for (int it = 0; it < 2; ++it) {
            int ch = it * NT1 + tid;
            wv[it] = w2[ch];
            int node = nb0 + (ch >> 4);
            int nodec = node < N ? node : N - 1;
            hv[it] = *(const u32x4*)(h + (size_t)nodec * D + (ch & 15) * 8);
        }
#pragma unroll
        for (int it = 0; it < 2; ++it) {
            int ch = it * NT1 + tid;
            *(u32x4*)&sW[(ch >> 4) * WST + (ch & 15) * 8] = wv[it];
            *(u32x4*)&sH[(ch >> 4) * WST + (ch & 15) * 8] = hv[it];
        }
        __syncthreads();
        STAMP(0, 1);
        const int np = wave & 3, mg = wave >> 2;
        bf16x8 hb[2][4];
#pragma unroll
        for (int m = 0; m < 2; ++m)
#pragma unroll
            for (int ks = 0; ks < 4; ++ks) hb[m][ks] = *(const bf16x8*)&sH[(mg * 32 + m * 16 + lg) * WST + ks * 32 + grp * 8];
        f32x4 acc[2][2];
#pragma unroll
        for (int m = 0; m < 2; ++m) { acc[m][0] = (f32x4){0.f, 0.f, 0.f, 0.f}; acc[m][1] = (f32x4){0.f, 0.f, 0.f, 0.f}; }
#pragma unroll
        for (int q = 0; q < 2; ++q) {
            int c = perm_ch(np * 2 + q, lg);
#pragma unroll
            for (int ks = 0; ks < 4; ++ks) {
                bf16x8 wf;
                int k0 = ks * 32 + grp * 8;
#pragma unroll
                for (int j = 0; j < 8; ++j) wf[j] = (short)sW[(k0 + j) * WST + c];
                acc[0][q] = __builtin_amdgcn_mfma_f32_16x16x32_bf16(wf, hb[0][ks], acc[0][q], 0, 0, 0);
                acc[1][q] = __builtin_amdgcn_mfma_f32_16x16x32_bf16(wf, hb[1][ks], acc[1][q], 0, 0, 0);
            }
        }
#pragma unroll
        for (int m = 0; m < 2; ++m) {
            int node = nb0 + mg * 32 + m * 16 + lg;
            if (node < N) {
                u32x4 v;
                v.x = pack2(acc[m][0][0], acc[m][0][1]); v.y = pack2(acc[m][0][2], acc[m][0][3]);
                v.z = pack2(acc[m][1][0], acc[m][1][1]); v.w = pack2(acc[m][1][2], acc[m][1][3]);
                *(u32x4*)(Bt + (size_t)node * D + grp * 32 + np * 8) = v;
            }
        }
        STAMP(0, 2);
        STAMP_FLUSH(0);
        return;
    }
    {
        int t = (bid - NC - NG) * NT1 + tid;
        if (t < 96 * 64) {
            int f = t >> 6;
            const unsigned short* W;
            int nt, ks;
            if (f < 32) { W = w_e; nt = f >> 2; ks = f & 3; }
            else { int g = f - 32; W = w_h; nt = g >> 3; ks = g & 7; }
            int c = perm_ch(nt, lg);
            int k0 = ks * 32 + 8 * grp;
            unsigned v[8];
#pragma unroll
            for (int j = 0; j < 8; ++j) v[j] = W[(size_t)(k0 + j) * D + c];
            u32x4 o;
            o.x = v[0] | (v[1] << 16); o.y = v[2] | (v[3] << 16); o.z = v[4] | (v[5] << 16); o.w = v[6] | (v[7] << 16);
            frags[t] = o;
        } else {
            for (int n = t - 96 * 64; n < N; n += (gridDim.x - NC - NG) * NT1 - 96 * 64) {
                u32x2 o;
                o.x = (unsigned)pos[n * 3] | ((unsigned)pos[n * 3 + 1] << 16);
                o.y = (unsigned)pos[n * 3 + 2];
                pos4[n] = o;
            }
        }
    }
}

static constexpr int MST = D + 8;
static constexpr int PST = D + 16;
static constexpr int NU_MAX = POOL / 32;
static constexpr int NPR = NU_MAX + T;

__global__ __launch_bounds__(NT2) void k2_kernel(const unsigned short* __restrict__ h,
                                                 const unsigned short* __restrict__ pos,
                                                 const u32x2* __restrict__ e_attr,
                                                 const int* __restrict__ src, const int* __restrict__ dst,
                                                 const unsigned short* __restrict__ w_e,
                                                 const unsigned short* __restrict__ w_x,
                                                 const u32x4* __restrict__ cells,
                                                 const u32x4* __restrict__ frags, const unsigned short* __restrict__ Bt,
                                                 const u32x2* __restrict__ pos4,
                                                 unsigned short* __restrict__ out_h, unsigned short* __restrict__ out_pos,
                                                 int N, int E, int NC) {
    __shared__ __attribute__((aligned(16))) u32x4 sWh[64 * 64];
    __shared__ __attribute__((aligned(16))) float sProw[NPR * PST];
    __shared__ __attribute__((aligned(16))) float sPpos[NPR * 8];
    __shared__ __attribute__((aligned(16))) unsigned short sH[MT * 16 * MST];
    __shared__ __attribute__((aligned(16))) unsigned short sA[MT * 16 * MST - 8 * MST];
    __shared__ __attribute__((aligned(16))) u32x4 sRec[POOL];
    __shared__ __attribute__((aligned(16))) unsigned short sCf[3][POOL];
    __shared__ __attribute__((aligned(16))) unsigned short sDl[POOL + 32];
    __shared__ float sWx[D];
    __shared__ float sPosT[T * 3 + 8];
    __shared__ int sCnt[64], sOff[64];
    __shared__ int sUf[64], sUl[64];
    __shared__ int sMisc[4];
    unsigned* sKey = (unsigned*)sProw;

    const int tid = threadIdx.x;
    const int wave = __builtin_amdgcn_readfirstlane(tid >> 6);
    const int l = tid & 63;
    const int lg = l & 15, grp = l >> 4;
    const int b = blockIdx.x;
    const int n0 = b * T;
    STAMP_DECL
    STAMP(1, 0);

    const u32x4* cb = cells + (size_t)b * NC * CS;
    constexpr int MAXR = 3;
    u32x4 ra[MAXR];
#pragma unroll
    for (int it = 0; it < MAXR; ++it) {
        int s = it * NT2 + tid;
        int sc = s < NC * CS ? s : NC * CS - 1;
        ra[it] = cb[sc];
    }
    u32x4 w1f[8];
    if (wave >= 12) {
#pragma unroll
        for (int i = 0; i < 8; ++i) w1f[i] = frags[((wave & 3) * 8 + i) * 64 + l];
    }
    u32x4 hv = {0u, 0u, 0u, 0u};
    if (wave < MT * 4) {
        int node = n0 + (tid >> 4);
        int nodec = node < N ? node : N - 1;
        hv = *(const u32x4*)(h + (size_t)nodec * D + (tid & 15) * 8);
    }
    unsigned posu, wxu;
    {
        int i = n0 * 3 + (tid < T * 3 ? tid : 0);
        posu = pos[i < N * 3 ? i : N * 3 - 1];
        wxu = w_x[tid & (D - 1)];
    }
    unsigned wfr[2][5];
#pragma unroll
    for (int a = 0; a < 2; ++a) {
        const unsigned short* wp = w_e + (size_t)256 * D + (4 * a + grp) * 16 + lg;
#pragma unroll
        for (int k = 0; k < 5; ++k) wfr[a][k] = wp[k * D];
    }
    if (tid < 64) sCnt[tid] = 0;
    if (tid < 4) sMisc[tid] = 0;
    lds_barrier();
    STAMP(1, 6);

    int rank[MAXR];
    unsigned key[MAXR];
    u32x2 ps[MAXR];
    unsigned pfa[MAXR], pfb[MAXR];
#pragma unroll
    for (int it = 0; it < MAXR; ++it) {
        int s = it * NT2 + tid;
        int c = s >> 5, r = s & 31;
        unsigned cc = (unsigned)__builtin_amdgcn_ds_bpermute((l & 32) << 2, (int)ra[it].x);
        bool valid = c < NC && cc <= (unsigned)CC && r >= 1 && (unsigned)r <= cc;
        if (c < NC && cc > (unsigned)CC && r == 0) sMisc[0] = 1;
        ra[it].y = valid ? ra[it].y : 0u;
        ps[it] = pos4[ra[it].y];
        int dl = ra[it].x >> 16;
        key[it] = ((unsigned)dl << 18) | ((unsigned)(c * CH) + (ra[it].x & 0xffffu));
        rank[it] = -1;
        if (valid) rank[it] = atomicAdd(&sCnt[dl], 1) | (dl << 16);
    }
#pragma unroll
    for (int it = 0; it < MAXR; ++it) {
        const unsigned* brow = (const unsigned*)(Bt + (size_t)ra[it].y * D);
        pfa[it] = 0; pfb[it] = 0;
    }
    if (NC * CS > MAXR * NT2 && tid == 0) sMisc[0] = 1;
    STAMP(1, 7);
    if (wave < MT * 4) *(u32x4*)&sH[(tid >> 4) * MST + (tid & 15) * 8] = hv;
    if (tid < T * 3) sPosT[tid] = bf1((unsigned short)posu);
    if (tid < D) sWx[tid] = bf1((unsigned short)wxu);
    unsigned wf[2][3];
#pragma unroll
    for (int a = 0; a < 2; ++a) {
        wf[a][0] = wfr[a][0] | (wfr[a][1] << 16); wf[a][1] = wfr[a][2] | (wfr[a][3] << 16); wf[a][2] = wfr[a][4];
    }
    unsigned sel[2][4];
#pragma unroll
    for (int hf2 = 0; hf2 < 2; ++hf2)
#pragma unroll
        for (int w = 0; w < 4; ++w) {
            int k0 = grp * 8 + w * 2, tk = hf2 * 16 + lg;
            sel[hf2][w] = (k0 == tk ? 0x3f80u : 0u) | (k0 + 1 == tk ? 0x3f800000u : 0u);
        }
    lds_barrier();
    STAMP(1, 5);

    int round_pos = 0;
    float* macc = (float*)sWh;
    u32x4* sRaw = (u32x4*)(sKey + POOL + 64);
    u32x2* sRawP = (u32x2*)(sKey + POOL + 64 + POOL * 4);
    int total;
    bool slow;
    {
        int cntl = sCnt[l];
        int incl = wave_incl_scan(cntl);
        int excl = incl - cntl;
        total = __builtin_amdgcn_readlane(incl, 63);
        slow = sMisc[0] != 0 || total > POOL;
        if (!slow) {
#pragma unroll
            for (int it = 0; it < MAXR; ++it) {
                int dl = rank[it] >> 16;
                int o = __builtin_amdgcn_ds_bpermute((dl & 63) << 2, excl);
                if (rank[it] >= 0) {
                    int p = o + (rank[it] & 0xffff);
                    sKey[p] = key[it]; sRaw[p] = ra[it]; sRawP[p] = ps[it];
                }
            }
            if (wave == 0) sOff[l] = excl;
            if (wave == 1 && l < 4) sKey[total + l] = 0xffffffffu;
        }
    }
    STAMP(1, 9);
    auto rank_step = [&](int tid, int total) {
        if (tid < total) {
            u32x4 rr = sRaw[tid];
            u32x2 pp = sRawP[tid];
            unsigned ky = sKey[tid];
            int dl = rr.x >> 16;
            int o = sOff[dl], end = o + sCnt[dl];
            int j0 = o & ~3;
            int p = j0;
            for (int j = j0; j < end; j += 4) {
                u32x4 k4 = *(const u32x4*)&sKey[j];
                p += (k4.x < ky ? 1 : 0) + (k4.y < ky ? 1 : 0) + (k4.z < ky ? 1 : 0) + (k4.w < ky ? 1 : 0);
            }
            float d0 = rbf(sPosT[dl * 3 + 0] - bf_lo(pp.x));
            float d1 = rbf(sPosT[dl * 3 + 1] - bf_hi(pp.x));
            float d2 = rbf(sPosT[dl * 3 + 2] - bf_lo(pp.y));
            float q = rbf(rbf(d0 * d0) + rbf(d1 * d1) + rbf(d2 * d2));
            u32x4 rec;
            rec.x = (__float_as_uint(q) >> 16) | (rr.z << 16);
            rec.y = (rr.z >> 16) | (rr.w << 16);
            rec.z = (rr.w >> 16) | ((unsigned)dl << 16);
            rec.w = rr.y;
            sRec[p] = rec;
            sCf[0][p] = (unsigned short)(__float_as_uint(d0) >> 16);
            sCf[1][p] = (unsigned short)(__float_as_uint(d1) >> 16);
            sCf[2][p] = (unsigned short)(__float_as_uint(d2) >> 16);
            sDl[p] = (unsigned short)dl;
        }
        if (tid < 32) sDl[total + tid] = 0xffffu;
        if (tid >= 64 && tid < 64 + T) {
            int n = tid - 64;
            int o = sOff[n], cnt = sCnt[n];
            sUf[n] = o >> 5;
            sUl[n] = cnt > 0 ? (o + cnt - 1) >> 5 : -1;
        }
    };
    lds_barrier();
    STAMP(1, 10);
    if (!slow) rank_step(tid, total);
    if (wave >= 12) {
        const int np = wave & 3;
#pragma unroll
        for (int mt = 0; mt < MT; ++mt) {
            f32x4 a0 = {0.f, 0.f, 0.f, 0.f}, a1 = {0.f, 0.f, 0.f, 0.f};
#pragma unroll
            for (int ks = 0; ks < 4; ++ks) {
                bf16x8 hb = *(const bf16x8*)&sH[(mt * 16 + lg) * MST + ks * 32 + grp * 8];
                a0 = __builtin_amdgcn_mfma_f32_16x16x32_bf16(__builtin_bit_cast(bf16x8, w1f[ks]), hb, a0, 0, 0, 0);
                a1 = __builtin_amdgcn_mfma_f32_16x16x32_bf16(__builtin_bit_cast(bf16x8, w1f[4 + ks]), hb, a1, 0, 0, 0);
            }
            int row = mt * 16 + lg;
            if (row < T) {
                u32x4 v;
                v.x = pack2(a0[0], a0[1]); v.y = pack2(a0[2], a0[3]); v.z = pack2(a1[0], a1[1]); v.w = pack2(a1[2], a1[3]);
                *(u32x4*)&sA[row * MST + grp * 32 + np * 8] = v;
            }
        }
    }
    if (slow) {
        for (int i = tid; i < T * PST; i += NT2) macc[i] = 0.f;
    } else {
#pragma unroll
        for (int i = 0; i < 4; ++i) {
            unsigned lds_off = __builtin_amdgcn_readfirstlane((unsigned)(uintptr_t)(sWh + i * NT2 + wave * 64));
            const u32x4* gp = frags + 32 * 64 + i * NT2 + tid;
            asm volatile("s_mov_b32 m0, %0\n\ts_nop 0\n\tglobal_load_lds_dwordx4 %1, off" :: "s"(lds_off), "v"(gp) : "memory", "m0");
        }
    }
    STAMP(1, 11);
    for (;;) {
        int tid = threadIdx.x;
        asm volatile("" : "+v"(tid));
        const int l = tid & 63;
        const int lg = l & 15, grp = l >> 4;
        if (slow) {
            __syncthreads();
            if (tid < 64) sCnt[tid] = 0;
            if (tid == 0) sMisc[1] = 0;
            __syncthreads();
            int e = round_pos + tid;
            round_pos += NT2;
            int rk = -1, dl = 0;
            u32x4 rr = {0u, 0u, 0u, 0u};
            u32x2 pp = {0u, 0u};
            if (e < E) {
                dl = dst[e] - n0;
                if (dl >= 0 && dl < T) {
                    int s = src[e];
                    u32x2 ea = e_attr[e];
                    rr.x = (unsigned)dl << 16; rr.y = (unsigned)s; rr.z = ea.x; rr.w = ea.y;
                    pp = pos4[s];
                    rk = atomicAdd(&sCnt[dl], 1);
                }
            }
            __syncthreads();
            int cntl = sCnt[l];
            int incl = wave_incl_scan(cntl);
            int excl = incl - cntl;
            total = __builtin_amdgcn_readlane(incl, 63);
            int o = __builtin_amdgcn_ds_bpermute((dl & 63) << 2, excl);
            if (rk >= 0) { int p = o + rk; sKey[p] = ((unsigned)dl << 18) | (unsigned)tid; sRaw[p] = rr; sRawP[p] = pp; }
            if (wave == 0) sOff[l] = excl;
            if (wave == 1 && l < 4) sKey[total + l] = 0xffffffffu;
            __syncthreads();
            rank_step(tid, total);
        }
        lds_barrier();
#pragma unroll
        for (int it = 0; it < MAXR; ++it) { asm volatile("" :: "v"(pfa[it]), "v"(pfb[it])); pfa[it] = 0u; pfb[it] = 0u; }
        STAMP(1, 1);

        const int NW = ((total + 31) >> 5) * 2;
        int wsi = 0;
        WSTAMP(wsi); wsi++;
        int wn = 0;
        if (l == 0) wn = atomicAdd(&sMisc[1], 1);
        wn = __builtin_amdgcn_readfirstlane(wn);
        u32x4 recn = {0u, 0u, 0u, 0u};
        u32x4 Xn[2];
#pragma unroll
        for (int kc = 0; kc < 2; ++kc) Xn[kc] = (u32x4){0u, 0u, 0u, 0u};
        if (wn < NW) {
            int e = (wn >> 1) * 32 + lg;
            recn = sRec[e < total ? e : total - 1];
            const unsigned short* brow = Bt + (size_t)recn.w * D + (wn & 1) * 64 + grp * 8;
#pragma unroll
            for (int kc = 0; kc < 2; ++kc) Xn[kc] = *(const u32x4*)(brow + kc * 32);
        }
        while (wn < NW) {
            const int u = wn >> 1, hg = wn & 1;
            const int p0 = u * 32;
            unsigned S[2][4][2];
            u32x4 rec0 = recn, X0[2], rec1, X1[2];
#pragma unroll
            for (int kc = 0; kc < 2; ++kc) X0[kc] = Xn[kc];
            {
                int e = p0 + 16 + lg;
                rec1 = sRec[e < total ? e : total - 1];
                const unsigned short* brow = Bt + (size_t)rec1.w * D + hg * 64 + grp * 8;
#pragma unroll
                for (int kc = 0; kc < 2; ++kc) X1[kc] = *(const u32x4*)(brow + kc * 32);
            }
            if (l == 0) wn = atomicAdd(&sMisc[1], 1);
            wn = __builtin_amdgcn_readfirstlane(wn);
            const u32x4 Wf = {hg ? wf[1][0] : wf[0][0], hg ? wf[1][1] : wf[0][1], hg ? wf[1][2] : wf[0][2], 0u};
#pragma unroll
            for (int bt = 0; bt < 2; ++bt) {
                const u32x4 rec = bt == 0 ? rec0 : rec1;
                const unsigned short* arow = &sA[(rec.z >> 16) * MST + hg * 64 + grp * 8];
                unsigned f2 = rec.z & 0xffffu;
                f32x4 acc[4];
#pragma unroll
                for (int g = 0; g < 4; ++g) {
                    u32x4 F;
                    F.x = grp == g ? rec.x : 0u; F.y = grp == g ? rec.y : 0u; F.z = grp == g ? f2 : 0u; F.w = 0u;
                    u32x4 Y = *(const u32x4*)(arow + (g >> 1) * 32);
                    u32x4 Sl = {sel[g & 1][0], sel[g & 1][1], sel[g & 1][2], sel[g & 1][3]};
                    u32x4 Xv = bt == 0 ? X0[g >> 1] : X1[g >> 1];
                    f32x4 a = {0.f, 0.f, 0.f, 0.f};
                    a = __builtin_amdgcn_mfma_f32_16x16x32_bf16(__builtin_bit_cast(bf16x8, F), __builtin_bit_cast(bf16x8, Wf), a, 0, 0, 0);
                    a = __builtin_amdgcn_mfma_f32_16x16x32_bf16(__builtin_bit_cast(bf16x8, Y), __builtin_bit_cast(bf16x8, Sl), a, 0, 0, 0);
                    a = __builtin_amdgcn_mfma_f32_16x16x32_bf16(__builtin_bit_cast(bf16x8, Xv), __builtin_bit_cast(bf16x8, Sl), a, 0, 0, 0);
                    acc[g] = a;
                }
                if (bt == 1) {
                    __builtin_amdgcn_sched_barrier(0);
                    if (wn < NW) {
                        int e = (wn >> 1) * 32 + lg;
                        recn = sRec[e < total ? e : total - 1];
                        const unsigned short* brow = Bt + (size_t)recn.w * D + (wn & 1) * 64 + grp * 8;
#pragma unroll
                        for (int kc = 0; kc < 2; ++kc) Xn[kc] = *(const u32x4*)(brow + kc * 32);
                    }
                    __builtin_amdgcn_sched_barrier(0);
                }
#pragma unroll
                for (int g = 0; g < 4; ++g) {
#pragma unroll
                    for (int hh = 0; hh < 2; ++hh) {
                        f32x2 x = {acc[g][hh * 2], acc[g][hh * 2 + 1]};
                        f32x2 tt = x * -1.44269504088896340736f;
                        f32x2 ex = {__builtin_amdgcn_exp2f(tt[0]), __builtin_amdgcn_exp2f(tt[1])};
                        ex += 1.0f;
                        f32x2 rc = {__builtin_amdgcn_rcpf(ex[0]), __builtin_amdgcn_rcpf(ex[1])};
                        S[bt][g][hh] = cvt_pk(x * rc);
                    }
                }
                __builtin_amdgcn_sched_barrier(0);
            }
            WSTAMP(wsi); wsi++;
            {
                int l = threadIdx.x;
                asm volatile("" : "+v"(l));
                l &= 63;
                const int lg = l & 15, grp = l >> 4;
                int lastp = p0 + 31 < total ? p0 + 31 : total - 1;
                int nlo = __builtin_amdgcn_readfirstlane((int)sDl[p0]);
                int nhi = __builtin_amdgcn_readfirstlane((int)sDl[lastp]);
                u32x2 dv0 = *(const u32x2*)&sDl[p0 + 4 * grp];
                u32x2 dv1 = *(const u32x2*)&sDl[p0 + 16 + 4 * grp];
                int c = lg & 3;
                int ci = c > 0 ? c - 1 : 0;
                u32x2 cf0 = *(const u32x2*)&sCf[ci][p0 + 4 * grp];
                u32x2 cf1 = *(const u32x2*)&sCf[ci][p0 + 16 + 4 * grp];
                if (c == 0) { cf0 = (u32x2){0x3f803f80u, 0x3f803f80u}; cf1 = cf0; }
                float wxv[4];
#pragma unroll
                for (int j = 0; j < 4; ++j) wxv[j] = sWx[(hg * 4 + j) * 16 + lg];
                for (int base = nlo; base <= nhi; base += 4) {
                    unsigned tg = (unsigned)(base + (lg >> 2)) * 0x10001u;
                    u32x4 G;
                    G.x = eq_mask16(dv0.x, tg) & cf0.x; G.y = eq_mask16(dv0.y, tg) & cf0.y;
                    G.z = eq_mask16(dv1.x, tg) & cf1.x; G.w = eq_mask16(dv1.y, tg) & cf1.y;
                    int node = base + grp;
                    bool act = node <= nhi;
                    float* prow = &sProw[(u + node) * PST + hg * 64 + lg];
                    f32x4 M[4];
#pragma unroll
                    for (int j = 0; j < 4; ++j) {
                        u32x4 Sv = {S[0][j][0], S[0][j][1], S[1][j][0], S[1][j][1]};
                        f32x4 z = {0.f, 0.f, 0.f, 0.f};
                        M[j] = __builtin_amdgcn_mfma_f32_16x16x32_bf16(__builtin_bit_cast(bf16x8, G), __builtin_bit_cast(bf16x8, Sv), z, 0, 0, 0);
                    }
                    float q1 = 0.f, q2 = 0.f, q3 = 0.f;
#pragma unroll
                    for (int j = 0; j < 4; ++j) {
                        q1 = fmaf(M[j][1], wxv[j], q1); q2 = fmaf(M[j][2], wxv[j], q2); q3 = fmaf(M[j][3], wxv[j], q3);
                    }
                    q1 = row16_sum(q1); q2 = row16_sum(q2); q3 = row16_sum(q3);
                    if (act) {
#pragma unroll
                        for (int j = 0; j < 4; ++j) prow[j * 16] = M[j][0];
                        if (lg < 3) sPpos[(u + node) * 8 + hg * 4 + lg] = lg == 0 ? q1 : (lg == 1 ? q2 : q3);
                    }
                }
            }
            WSTAMP(wsi); wsi++;
        }
        STAMP(1, 2);
        asm volatile("s_waitcnt vmcnt(0)" ::: "memory");
        __syncthreads();
        STAMP(1, 3);
        if (!slow) break;
        tid = threadIdx.x;
        asm volatile("" : "+v"(tid));
        for (int i = tid; i < T * 32; i += NT2) {
            int n = i >> 5, c4 = i & 31;
            f32x4 a = *(const f32x4*)&macc[n * PST + c4 * 4];
            for (int uu = sUf[n]; uu <= sUl[n]; ++uu) a += *(const f32x4*)&sProw[(uu + n) * PST + c4 * 4];
            *(f32x4*)&macc[n * PST + c4 * 4] = a;
        }
        if (tid < T * 3) {
            int n = tid / 3, c = tid - n * 3;
            float a = macc[n * PST + D + c];
            for (int uu = sUf[n]; uu <= sUl[n]; ++uu) a += sPpos[(uu + n) * 8 + c] + sPpos[(uu + n) * 8 + 4 + c];
            macc[n * PST + D + c] = a;
        }
        if (round_pos >= E) break;
    }
    if (slow) {
        int tid = threadIdx.x;
        asm volatile("" : "+v"(tid));
        __syncthreads();
        for (int i = tid; i < T * 32; i += NT2) {
            int n = i >> 5, c4 = i & 31;
            *(f32x4*)&sProw[n * PST + c4 * 4] = *(const f32x4*)&macc[n * PST + c4 * 4];
        }
        if (tid < T * 3) { int n = tid / 3, c = tid - n * 3; sPpos[n * 8 + c] = macc[n * PST + D + c]; sPpos[n * 8 + 4 + c] = 0.f; }
        if (tid < T) { sUf[tid] = 0; sUl[tid] = 0; }
        __syncthreads();
#pragma unroll
        for (int i = 0; i < 4; ++i) sWh[i * NT2 + tid] = frags[32 * 64 + i * NT2 + tid];
        __syncthreads();
    }

    if (wave < MT * 4) {
        int l = threadIdx.x;
        asm volatile("" : "+v"(l));
        l &= 63;
        const int lg = l & 15, grp = l >> 4;
        int mt = wave >> 2, np = wave & 3;
        int row = mt * 16 + lg;
        int node = n0 + row;
        bool valid = (row < T) && (node < N);
        int uf = valid ? sUf[row] : 0, ul = valid ? sUl[row] : -1;
        f32x4 a0 = {0.f, 0.f, 0.f, 0.f}, a1 = {0.f, 0.f, 0.f, 0.f};
#pragma unroll
        for (int ks = 0; ks < 8; ++ks) {
            bf16x8 xb;
            if (ks < 4) xb = *(const bf16x8*)&sH[row * MST + ks * 32 + grp * 8];
            else {
                f32x4 m0 = {0.f, 0.f, 0.f, 0.f}, m1 = {0.f, 0.f, 0.f, 0.f};
                for (int uu = uf; uu <= ul; ++uu) {
                    const float* pr = &sProw[(uu + row) * PST + (ks - 4) * 32 + grp * 8];
                    m0 += *(const f32x4*)pr; m1 += *(const f32x4*)(pr + 4);
                }
                u32x4 v;
                v.x = cvt_pk((f32x2){m0[0], m0[1]}); v.y = cvt_pk((f32x2){m0[2], m0[3]});
                v.z = cvt_pk((f32x2){m1[0], m1[1]}); v.w = cvt_pk((f32x2){m1[2], m1[3]});
                xb = __builtin_bit_cast(bf16x8, v);
            }
            a0 = __builtin_amdgcn_mfma_f32_16x16x32_bf16(__builtin_bit_cast(bf16x8, sWh[((np * 2) * 8 + ks) * 64 + l]), xb, a0, 0, 0, 0);
            a1 = __builtin_amdgcn_mfma_f32_16x16x32_bf16(__builtin_bit_cast(bf16x8, sWh[((np * 2 + 1) * 8 + ks) * 64 + l]), xb, a1, 0, 0, 0);
        }
        if (valid) {
            u32x4 hv2 = *(const u32x4*)&sH[row * MST + grp * 32 + np * 8];
            u32x4 v;
            v.x = pack2(bf_lo(hv2.x) + silu_f(a0[0]), bf_hi(hv2.x) + silu_f(a0[1]));
            v.y = pack2(bf_lo(hv2.y) + silu_f(a0[2]), bf_hi(hv2.y) + silu_f(a0[3]));
            v.z = pack2(bf_lo(hv2.z) + silu_f(a1[0]), bf_hi(hv2.z) + silu_f(a1[1]));
            v.w = pack2(bf_lo(hv2.w) + silu_f(a1[2]), bf_hi(hv2.w) + silu_f(a1[3]));
            *(u32x4*)(out_h + (size_t)node * D + grp * 32 + np * 8) = v;
        }
    } else {
        int t = threadIdx.x;
        asm volatile("" : "+v"(t));
        t -= MT * 4 * 64;
        if (t < T * 3 && n0 * 3 + t < N * 3) {
            int n = t / 3, c = t - n * 3;
            float a = 0.f;
            for (int uu = sUf[n]; uu <= sUl[n]; ++uu) a += sPpos[(uu + n) * 8 + c] + sPpos[(uu + n) * 8 + 4 + c];
            out_pos[(size_t)n0 * 3 + t] = (unsigned short)f2bf(sPosT[t] + a);
        }
    }
    STAMP(1, 4);
    STAMP_FLUSH(1);
    WSTAMP_FLUSH();
}

extern "C" void kernel_launch(void* const* d_in, const int* in_sizes, int n_in,
                              void* d_out, int out_size, void* d_ws, size_t ws_size,
                              hipStream_t stream) {
    const unsigned short* h = (const unsigned short*)d_in[0];
    const unsigned short* pos = (const unsigned short*)d_in[1];
    const u32x2* e_attr = (const u32x2*)d_in[2];
    const int* src = (const int*)d_in[3];
    const int* dst = (const int*)d_in[4];
    const unsigned short* w_e = (const unsigned short*)d_in[5];
    const unsigned short* w_x = (const unsigned short*)d_in[6];
    const unsigned short* w_h = (const unsigned short*)d_in[7];
    const int N = in_sizes[1] / 3;
    const int E = in_sizes[3];
    const int NB = (N + T - 1) / T;
    const int NC = (E + CH - 1) / CH;
    const int NG = (N + 127) / 128;

    char* ws = (char*)d_ws;
    size_t off = 0;
    auto take = [&](size_t bytes) { char* p = ws + off; off += (bytes + 255) / 256 * 256; return p; };
    u32x4* frags = (u32x4*)take((size_t)96 * 64 * 16);
    unsigned short* Bt = (unsigned short*)take((size_t)N * D * 2);
    u32x2* pos4 = (u32x2*)take((size_t)N * 8);
    u32x4* cells = (u32x4*)take((size_t)NB * NC * CS * 16);

    unsigned short* out_h = (unsigned short*)d_out;
    unsigned short* out_pos = out_h + (size_t)N * D;

    size_t lds1 = (size_t)CH * 20 + (size_t)NB * 8;
    if (lds1 < (size_t)2 * 128 * (D + 8) * 2) lds1 = (size_t)2 * 128 * (D + 8) * 2;
    (void)hipFuncSetAttribute((const void*)k1_kernel, hipFuncAttributeMaxDynamicSharedMemorySize, (int)lds1);
    k1_kernel<<<NC + NG + 16, NT1, lds1, stream>>>(h, pos, e_attr, src, dst, w_e, w_h, cells, frags, Bt, pos4,
                                                    N, E, NB, NC, NG);
    k2_kernel<<<NB, NT2, 0, stream>>>(h, pos, e_attr, src, dst, w_e, w_x, cells, frags, Bt, pos4,
                                      out_h, out_pos, N, E, NC);
}
```

```cpp
#include <hip/hip_runtime.h>
#include <hip/hip_bf16.h>
#include <stdint.h>

typedef __attribute__((ext_vector_type(8))) short bf16x8;
typedef __attribute__((ext_vector_type(4))) float f32x4;
typedef __attribute__((ext_vector_type(2))) float f32x2;
typedef __attribute__((ext_vector_type(4))) unsigned u32x4;
typedef __attribute__((ext_vector_type(2))) unsigned u32x2;
typedef __attribute__((ext_vector_type(2))) __bf16 bf16x2v;
typedef __attribute__((ext_vector_type(2))) unsigned short u16x2;

#define STAMP_DECL
#define STAMP(K, i)
#define STAMP_FLUSH(K)
#define WSTAMP(i)
#define WSTAMP_FLUSH()

static constexpr int D = 128;
static constexpr int T = 40;
static constexpr int MT = 3;
static constexpr int POOL = 1024;
static constexpr int CH = 2048;
static constexpr int CS = 32;
static constexpr int CC = CS - 1;
static constexpr int NT1 = 1024;
static constexpr int NT2 = 1024;

static constexpr float KSC = -1.44269504088896340736f;
static constexpr float KIN = -0.69314718055994530942f;

__device__ __forceinline__ float bf_lo(unsigned u) { return __uint_as_float(u << 16); }
__device__ __forceinline__ float bf_hi(unsigned u) { return __uint_as_float(u & 0xffff0000u); }
__device__ __forceinline__ float bf1(unsigned short u) { return __uint_as_float(((unsigned)u) << 16); }
__device__ __forceinline__ unsigned f2bf(float f) {
    __hip_bfloat16 b = __float2bfloat16(f);
    return (unsigned)*reinterpret_cast<unsigned short*>(&b);
}
__device__ __forceinline__ float rbf(float f) { return __uint_as_float(f2bf(f) << 16); }
__device__ __forceinline__ unsigned pack2(float lo, float hi) { return f2bf(lo) | (f2bf(hi) << 16); }

__device__ __forceinline__ unsigned cvt_pk(f32x2 v) {
    return __builtin_bit_cast(unsigned, __builtin_convertvector(v, bf16x2v));
}
__device__ __forceinline__ unsigned eq_sel16(unsigned a, unsigned b, unsigned c) {
    unsigned d = a ^ b, m;
    asm("v_pk_min_u16 %0, %1, %2" : "=v"(m) : "v"(d), "v"(0x00010001u));
    asm("v_pk_sub_u16 %0, %1, %2" : "=v"(m) : "v"(m), "v"(0x00010001u));
    return m & c;
}
__device__ __forceinline__ void lds_barrier() {
    asm volatile("s_waitcnt lgkmcnt(0)" ::: "memory");
    __builtin_amdgcn_s_barrier();
    asm volatile("" ::: "memory");
}
__device__ __forceinline__ float silu_f(float x) {
    float e = __builtin_amdgcn_exp2f(x * -1.44269504088896340736f);
    return x * __builtin_amdgcn_rcpf(1.0f + e);
}

template <int CTRL> __device__ __forceinline__ float dpp_mov(float v) {
    return __builtin_bit_cast(float, __builtin_amdgcn_update_dpp(0, __builtin_bit_cast(int, v), CTRL, 0xf, 0xf, false));
}
__device__ __forceinline__ float row16_sum(float v) {
    v += dpp_mov<0x128>(v);
    v += dpp_mov<0x124>(v);
    v += dpp_mov<0x122>(v);
    v += dpp_mov<0x121>(v);
    return v;
}
__device__ __forceinline__ int wave_incl_scan(int x) {
    x += __builtin_amdgcn_update_dpp(0, x, 0x111, 0xf, 0xf, false);
    x += __builtin_amdgcn_update_dpp(0, x, 0x112, 0xf, 0xf, false);
    x += __builtin_amdgcn_update_dpp(0, x, 0x114, 0xf, 0xf, false);
    x += __builtin_amdgcn_update_dpp(0, x, 0x118, 0xf, 0xf, false);
    x += __builtin_amdgcn_update_dpp(0, x, 0x142, 0xa, 0xf, false);
    x += __builtin_amdgcn_update_dpp(0, x, 0x143, 0xc, 0xf, false);
    return x;
}

__device__ __forceinline__ int perm_ch(int nt, int i) { return (i >> 2) * 32 + nt * 4 + (i & 3); }

__global__ __launch_bounds__(NT1) void k1_kernel(const unsigned short* __restrict__ h,
                                                 const unsigned short* __restrict__ pos,
                                                 const u32x2* __restrict__ e_attr,
                                                 const int* __restrict__ src, const int* __restrict__ dst,
                                                 const unsigned short* __restrict__ w_e,
                                                 const unsigned short* __restrict__ w_h,
                                                 u32x4* __restrict__ cells, u32x4* __restrict__ frags,
                                                 unsigned short* __restrict__ Bt, u32x2* __restrict__ pos4,
                                                 int N, int E, int NB, int NC, int NG) {
    extern __shared__ __attribute__((aligned(16))) char smem[];
    const int tid = threadIdx.x;
    const int bid = blockIdx.x;
    STAMP_DECL
    STAMP(0, 0);
    if (bid < NC) {
        u32x4* sRA = (u32x4*)smem;
        int* sBk = (int*)(smem + CH * 16);
        int* lcnt = (int*)(smem + CH * 20);
        int* loff = lcnt + NB;
        const int ebase = bid * CH;
        int dd[CH / NT1], ss[CH / NT1]; u32x2 ea[CH / NT1];
#pragma unroll
        for (int it = 0; it < CH / NT1; ++it) {
            int e = ebase + it * NT1 + tid;
            int ec = e < E ? e : E - 1;
            dd[it] = dst[ec]; ss[it] = src[ec]; ea[it] = e_attr[ec];
        }
        for (int i = tid; i < NB; i += NT1) lcnt[i] = 0;
        __syncthreads();
        STAMP(0, 3);
        u32x4 ra[CH / NT1]; int bk[CH / NT1], rk[CH / NT1];
#pragma unroll
        for (int it = 0; it < CH / NT1; ++it) {
            int el = it * NT1 + tid;
            int e = ebase + el;
            bk[it] = -1;
            if (e < E) {
                int d = dd[it];
                int b = d / T;
                int dl = d - b * T;
                rk[it] = atomicAdd(&lcnt[b], 1);
                bk[it] = b;
                ra[it].x = ((unsigned)dl << 16) | (unsigned)el; ra[it].y = (unsigned)ss[it]; ra[it].z = ea[it].x; ra[it].w = ea[it].y;
            }
        }
        __syncthreads();
        STAMP(0, 4);
        if (tid < 64) {
            int carry = 0;
            for (int base = 0; base < NB; base += 64) {
                int i = base + tid;
                int v = i < NB ? lcnt[i] : 0;
                int x = wave_incl_scan(v);
                if (i < NB) loff[i] = carry + x - v;
                carry += __builtin_amdgcn_readlane(x, 63);
            }
        }
        __syncthreads();
        STAMP(0, 5);
#pragma unroll
        for (int it = 0; it < CH / NT1; ++it) {
            if (bk[it] >= 0) {
                int p = loff[bk[it]] + rk[it];
                sRA[p] = ra[it]; sBk[p] = bk[it];
            }
        }
        __syncthreads();
        STAMP(0, 1);
        const int nvalid = (E - ebase) < CH ? (E - ebase) : CH;
#pragma unroll
        for (int it = 0; it < CH / NT1; ++it) {
            int p = it * NT1 + tid;
            if (p < nvalid) {
                int b = sBk[p];
                int r = p - loff[b];
                if (r < CC) cells[((size_t)b * NC + bid) * CS + 1 + r] = sRA[p];
            }
        }
        for (int i = tid; i < NB; i += NT1) *(int*)(cells + ((size_t)i * NC + bid) * CS) = lcnt[i];
        STAMP(0, 2);
        STAMP_FLUSH(0);
        return;
    }
    const int wave = tid >> 6, l = tid & 63;
    const int lg = l & 15, grp = l >> 4;
    if (bid < NC + NG) {
        constexpr int WST = D + 8;
        unsigned short* sW = (unsigned short*)smem;
        unsigned short* sH = sW + 128 * WST;
        const int nb0 = (bid - NC) * 128;
        const u32x4* w2 = (const u32x4*)(w_e + (size_t)128 * D);
        u32x4 wv[2], hv[2];
#pragma unroll
        for (int it = 0; it < 2; ++it) {
            int ch = it * NT1 + tid;
            wv[it] = w2[ch];
            int node = nb0 + (ch >> 4);
            int nodec = node < N ? node : N - 1;
            hv[it] = *(const u32x4*)(h + (size_t)nodec * D + (ch & 15) * 8);
        }
#pragma unroll
        for (int it = 0; it < 2; ++it) {
            int ch = it * NT1 + tid;
            *(u32x4*)&sW[(ch >> 4) * WST + (ch & 15) * 8] = wv[it];
            *(u32x4*)&sH[(ch >> 4) * WST + (ch & 15) * 8] = hv[it];
        }
        __syncthreads();
        STAMP(0, 1);
        const int np = wave & 3, mg = wave >> 2;
        bf16x8 hb[2][4];
#pragma unroll
        for (int m = 0; m < 2; ++m)
#pragma unroll
            for (int ks = 0; ks < 4; ++ks) hb[m][ks] = *(const bf16x8*)&sH[(mg * 32 + m * 16 + lg) * WST + ks * 32 + grp * 8];
        f32x4 acc[2][2];
#pragma unroll
        for (int m = 0; m < 2; ++m) { acc[m][0] = (f32x4){0.f, 0.f, 0.f, 0.f}; acc[m][1] = (f32x4){0.f, 0.f, 0.f, 0.f}; }
#pragma unroll
        for (int q = 0; q < 2; ++q) {
            int c = perm_ch(np * 2 + q, lg);
#pragma unroll
            for (int ks = 0; ks < 4; ++ks) {
                bf16x8 wf;
                int k0 = ks * 32 + grp * 8;
#pragma unroll
                for (int j = 0; j < 8; ++j) wf[j] = (short)sW[(k0 + j) * WST + c];
                acc[0][q] = __builtin_amdgcn_mfma_f32_16x16x32_bf16(wf, hb[0][ks], acc[0][q], 0, 0, 0);
                acc[1][q] = __builtin_amdgcn_mfma_f32_16x16x32_bf16(wf, hb[1][ks], acc[1][q], 0, 0, 0);
            }
        }
#pragma unroll
        for (int m = 0; m < 2; ++m) {
            int node = nb0 + mg * 32 + m * 16 + lg;
            if (node < N) {
                u32x4 v;
                v.x = pack2(acc[m][0][0] * KSC, acc[m][0][1] * KSC); v.y = pack2(acc[m][0][2] * KSC, acc[m][0][3] * KSC);
                v.z = pack2(acc[m][1][0] * KSC, acc[m][1][1] * KSC); v.w = pack2(acc[m][1][2] * KSC, acc[m][1][3] * KSC);
                *(u32x4*)(Bt + (size_t)node * D + grp * 32 + np * 8) = v;
            }
        }
        STAMP(0, 2);
        STAMP_FLUSH(0);
        return;
    }
    {
        int t = (bid - NC - NG) * NT1 + tid;
        if (t < 96 * 64) {
            int f = t >> 6;
            const unsigned short* W;
            int nt, ks;
            if (f < 32) { W = w_e; nt = f >> 2; ks = f & 3; }
            else { int g = f - 32; W = w_h; nt = g >> 3; ks = g & 7; }
            int c = perm_ch(nt, lg);
            int k0 = ks * 32 + 8 * grp;
            unsigned v[8];
#pragma unroll
            for (int j = 0; j < 8; ++j) v[j] = W[(size_t)(k0 + j) * D + c];
            u32x4 o;
            o.x = v[0] | (v[1] << 16); o.y = v[2] | (v[3] << 16); o.z = v[4] | (v[5] << 16); o.w = v[6] | (v[7] << 16);
            frags[t] = o;
        } else if (t < 104 * 64) {
            int f = (t >> 6) - 96;
            const unsigned short* wp = w_e + (size_t)256 * D + f * 16 + lg;
            unsigned v[5];
#pragma unroll
            for (int k = 0; k < 5; ++k) {
                float w = bf1(wp[k * D]) * KSC;
                float hi = rbf(w);
                v[k] = grp == 0 ? f2bf(hi) : (grp == 1 ? f2bf(w - hi) : 0u);
            }
            u32x4 o;
            o.x = v[0] | (v[1] << 16); o.y = v[2] | (v[3] << 16); o.z = v[4]; o.w = 0u;
            frags[t] = o;
        } else {
            for (int n = t - 104 * 64; n < N; n += (gridDim.x - NC - NG) * NT1 - 104 * 64) {
                u32x2 o;
                o.x = (unsigned)pos[n * 3] | ((unsigned)pos[n * 3 + 1] << 16);
                o.y = (unsigned)pos[n * 3 + 2];
                pos4[n] = o;
            }
        }
    }
}

static constexpr int MST = D + 8;
static constexpr int PST = D + 16;
static constexpr int NU_MAX = POOL / 32;
static constexpr int NPR = NU_MAX + T;

__global__ __launch_bounds__(NT2) void k2_kernel(const unsigned short* __restrict__ h,
                                                 const unsigned short* __restrict__ pos,
                                                 const u32x2* __restrict__ e_attr,
                                                 const int* __restrict__ src, const int* __restrict__ dst,
                                                 const unsigned short* __restrict__ w_e,
                                                 const unsigned short* __restrict__ w_x,
                                                 const u32x4* __restrict__ cells,
                                                 const u32x4* __restrict__ frags, const unsigned short* __restrict__ Bt,
                                                 const u32x2* __restrict__ pos4,
                                                 unsigned short* __restrict__ out_h, unsigned short* __restrict__ out_pos,
                                                 int N, int E, int NC) {
    __shared__ __attribute__((aligned(16))) u32x4 sWh[64 * 64];
    __shared__ __attribute__((aligned(16))) float sProw[NPR * PST];
    __shared__ __attribute__((aligned(16))) float sPpos[NPR * 8];
    __shared__ __attribute__((aligned(16))) unsigned short sH[MT * 16 * MST];
    __shared__ __attribute__((aligned(16))) unsigned short sA[MT * 16 * MST - 8 * MST];
    __shared__ __attribute__((aligned(16))) u32x4 sRec[POOL];
    __shared__ __attribute__((aligned(16))) unsigned short sCf[3][POOL];
    __shared__ __attribute__((aligned(16))) unsigned short sDl[POOL + 32];
    __shared__ float sWx[D];
    __shared__ float sPosT[T * 3 + 8];
    __shared__ int sCnt[64], sOff[64];
    __shared__ int sUf[64], sUl[64];
    __shared__ int sMisc[4];
    unsigned* sKey = (unsigned*)sProw;

    const int tid = threadIdx.x;
    const int wave = __builtin_amdgcn_readfirstlane(tid >> 6);
    const int l = tid & 63;
    const int lg = l & 15, grp = l >> 4;
    const int b = blockIdx.x;
    const int n0 = b * T;
    STAMP_DECL
    STAMP(1, 0);

    const u32x4* cb = cells + (size_t)b * NC * CS;
    constexpr int MAXR = 3;
    u32x4 ra[MAXR];
#pragma unroll
    for (int it = 0; it < MAXR; ++it) {
        int s = it * NT2 + tid;
        int sc = s < NC * CS ? s : NC * CS - 1;
        ra[it] = cb[sc];
    }
    u32x4 w1f[8];
    if (wave >= 12) {
#pragma unroll
        for (int i = 0; i < 8; ++i) w1f[i] = frags[((wave & 3) * 8 + i) * 64 + l];
    }
    u32x4 hv = {0u, 0u, 0u, 0u};
    if (wave < MT * 4) {
        int node = n0 + (tid >> 4);
        int nodec = node < N ? node : N - 1;
        hv = *(const u32x4*)(h + (size_t)nodec * D + (tid & 15) * 8);
    }
    unsigned posu, wxu;
    {
        int i = n0 * 3 + (tid < T * 3 ? tid : 0);
        posu = pos[i < N * 3 ? i : N * 3 - 1];
        wxu = w_x[tid & (D - 1)];
    }
    u32x4 wf[4];
#pragma unroll
    for (int i = 0; i < 4; ++i) wf[i] = frags[(96 + (wave & 1) * 4 + i) * 64 + l];
    if (tid < 64) sCnt[tid] = 0;
    if (tid < 4) sMisc[tid] = 0;
    lds_barrier();
    STAMP(1, 6);

    int rank[MAXR];
    unsigned key[MAXR];
    u32x2 ps[MAXR];
    unsigned pfa[MAXR], pfb[MAXR];
#pragma unroll
    for (int it = 0; it < MAXR; ++it) {
        int s = it * NT2 + tid;
        int c = s >> 5, r = s & 31;
        unsigned cc = (unsigned)__builtin_amdgcn_ds_bpermute((l & 32) << 2, (int)ra[it].x);
        bool valid = c < NC && cc <= (unsigned)CC && r >= 1 && (unsigned)r <= cc;
        if (c < NC && cc > (unsigned)CC && r == 0) sMisc[0] = 1;
        ra[it].y = valid ? ra[it].y : 0u;
        ps[it] = pos4[ra[it].y];
        int dl = ra[it].x >> 16;
        key[it] = ((unsigned)dl << 18) | ((unsigned)(c * CH) + (ra[it].x & 0xffffu));
        rank[it] = -1;
        if (valid) rank[it] = atomicAdd(&sCnt[dl], 1) | (dl << 16);
    }
#pragma unroll
    for (int it = 0; it < MAXR; ++it) {
        const unsigned* brow = (const unsigned*)(Bt + (size_t)ra[it].y * D);
        pfa[it] = 0; pfb[it] = 0;
    }
    if (NC * CS > MAXR * NT2 && tid == 0) sMisc[0] = 1;
    STAMP(1, 7);
    if (wave < MT * 4) *(u32x4*)&sH[(tid >> 4) * MST + (tid & 15) * 8] = hv;
    if (tid < T * 3) sPosT[tid] = bf1((unsigned short)posu);
    if (tid < D) sWx[tid] = bf1((unsigned short)wxu) * KIN;
    unsigned sel[2][4];
#pragma unroll
    for (int hf2 = 0; hf2 < 2; ++hf2)
#pragma unroll
        for (int w = 0; w < 4; ++w) {
            int k0 = grp * 8 + w * 2, tk = hf2 * 16 + lg;
            sel[hf2][w] = (k0 == tk ? 0x3f80u : 0u) | (k0 + 1 == tk ? 0x3f800000u : 0u);
        }
    lds_barrier();
    STAMP(1, 5);

    int round_pos = 0;
    float* macc = (float*)sWh;
    u32x4* sRaw = (u32x4*)(sKey + POOL + 64);
    u32x2* sRawP = (u32x2*)(sKey + POOL + 64 + POOL * 4);
    int total;
    bool slow;
    {
        int cntl = sCnt[l];
        int incl = wave_incl_scan(cntl);
        int excl = incl - cntl;
        total = __builtin_amdgcn_readlane(incl, 63);
        slow = sMisc[0] != 0 || total > POOL;
        if (!slow) {
#pragma unroll
            for (int it = 0; it < MAXR; ++it) {
                int dl = rank[it] >> 16;
                int o = __builtin_amdgcn_ds_bpermute((dl & 63) << 2, excl);
                if (rank[it] >= 0) {
                    int p = o + (rank[it] & 0xffff);
                    sKey[p] = key[it]; sRaw[p] = ra[it]; sRawP[p] = ps[it];
                }
            }
            if (wave == 0) sOff[l] = excl;
            if (wave == 1 && l < 4) sKey[total + l] = 0xffffffffu;
        }
    }
    STAMP(1, 9);
    auto rank_step = [&](int tid, int total) {
        if (tid < total) {
            u32x4 rr = sRaw[tid];
            u32x2 pp = sRawP[tid];
            unsigned ky = sKey[tid];
            int dl = rr.x >> 16;
            int o = sOff[dl], end = o + sCnt[dl];
            int j0 = o & ~3;
            int p = j0;
            for (int j = j0; j < end; j += 4) {
                u32x4 k4 = *(const u32x4*)&sKey[j];
                p += (k4.x < ky ? 1 : 0) + (k4.y < ky ? 1 : 0) + (k4.z < ky ? 1 : 0) + (k4.w < ky ? 1 : 0);
            }
            float d0 = rbf(sPosT[dl * 3 + 0] - bf_lo(pp.x));
            float d1 = rbf(sPosT[dl * 3 + 1] - bf_hi(pp.x));
            float d2 = rbf(sPosT[dl * 3 + 2] - bf_lo(pp.y));
            float q = rbf(rbf(d0 * d0) + rbf(d1 * d1) + rbf(d2 * d2));
            u32x4 rec;
            rec.x = (__float_as_uint(q) >> 16) | (rr.z << 16);
            rec.y = (rr.z >> 16) | (rr.w << 16);
            rec.z = (rr.w >> 16) | ((unsigned)dl << 16);
            rec.w = rr.y;
            sRec[p] = rec;
            sCf[0][p] = (unsigned short)(__float_as_uint(d0) >> 16);
            sCf[1][p] = (unsigned short)(__float_as_uint(d1) >> 16);
            sCf[2][p] = (unsigned short)(__float_as_uint(d2) >> 16);
            sDl[p] = (unsigned short)dl;
        }
        if (tid < 32) sDl[total + tid] = 0xffffu;
        if (tid >= 64 && tid < 64 + T) {
            int n = tid - 64;
            int o = sOff[n], cnt = sCnt[n];
            sUf[n] = o >> 5;
            sUl[n] = cnt > 0 ? (o + cnt - 1) >> 5 : -1;
        }
    };
    lds_barrier();
    STAMP(1, 10);
    if (!slow) rank_step(tid, total);
    if (wave >= 12) {
        const int np = wave & 3;
#pragma unroll
        for (int mt = 0; mt < MT; ++mt) {
            f32x4 a0 = {0.f, 0.f, 0.f, 0.f}, a1 = {0.f, 0.f, 0.f, 0.f};
#pragma unroll
            for (int ks = 0; ks < 4; ++ks) {
                bf16x8 hb = *(const bf16x8*)&sH[(mt * 16 + lg) * MST + ks * 32 + grp * 8];
                a0 = __builtin_amdgcn_mfma_f32_16x16x32_bf16(__builtin_bit_cast(bf16x8, w1f[ks]), hb, a0, 0, 0, 0);
                a1 = __builtin_amdgcn_mfma_f32_16x16x32_bf16(__builtin_bit_cast(bf16x8, w1f[4 + ks]), hb, a1, 0, 0, 0);
            }
            int row = mt * 16 + lg;
            if (row < T) {
                u32x4 v;
                v.x = pack2(a0[0] * KSC, a0[1] * KSC); v.y = pack2(a0[2] * KSC, a0[3] * KSC);
                v.z = pack2(a1[0] * KSC, a1[1] * KSC); v.w = pack2(a1[2] * KSC, a1[3] * KSC);
                *(u32x4*)&sA[row * MST + grp * 32 + np * 8] = v;
            }
        }
    }
    if (slow) {
        for (int i = tid; i < T * PST; i += NT2) macc[i] = 0.f;
    } else {
#pragma unroll
        for (int i = 0; i < 4; ++i) {
            unsigned lds_off = __builtin_amdgcn_readfirstlane((unsigned)(uintptr_t)(sWh + i * NT2 + wave * 64));
            const u32x4* gp = frags + 32 * 64 + i * NT2 + tid;
            asm volatile("s_mov_b32 m0, %0\n\ts_nop 0\n\tglobal_load_lds_dwordx4 %1, off" :: "s"(lds_off), "v"(gp) : "memory", "m0");
        }
    }
    STAMP(1, 11);
    for (;;) {
        int tid = threadIdx.x;
        asm volatile("" : "+v"(tid));
        const int l = tid & 63;
        const int lg = l & 15, grp = l >> 4;
        if (slow) {
            __syncthreads();
            if (tid < 64) sCnt[tid] = 0;
            if (tid == 0) sMisc[1] = 0;
            __syncthreads();
            int e = round_pos + tid;
            round_pos += NT2;
            int rk = -1, dl = 0;
            u32x4 rr = {0u, 0u, 0u, 0u};
            u32x2 pp = {0u, 0u};
            if (e < E) {
                dl = dst[e] - n0;
                if (dl >= 0 && dl < T) {
                    int s = src[e];
                    u32x2 ea = e_attr[e];
                    rr.x = (unsigned)dl << 16; rr.y = (unsigned)s; rr.z = ea.x; rr.w = ea.y;
                    pp = pos4[s];
                    rk = atomicAdd(&sCnt[dl], 1);
                }
            }
            __syncthreads();
            int cntl = sCnt[l];
            int incl = wave_incl_scan(cntl);
            int excl = incl - cntl;
            total = __builtin_amdgcn_readlane(incl, 63);
            int o = __builtin_amdgcn_ds_bpermute((dl & 63) << 2, excl);
            if (rk >= 0) { int p = o + rk; sKey[p] = ((unsigned)dl << 18) | (unsigned)tid; sRaw[p] = rr; sRawP[p] = pp; }
            if (wave == 0) sOff[l] = excl;
            if (wave == 1 && l < 4) sKey[total + l] = 0xffffffffu;
            __syncthreads();
            rank_step(tid, total);
        }
        lds_barrier();
#pragma unroll
        for (int it = 0; it < MAXR; ++it) { asm volatile("" :: "v"(pfa[it]), "v"(pfb[it])); pfa[it] = 0u; pfb[it] = 0u; }
        STAMP(1, 1);

        const int NW = ((total + 31) >> 5) * 2;
        int wsi = 0;
        WSTAMP(wsi); wsi++;
        auto fetch = [&](int g, u32x4 (&R)[2], u32x4 (&X)[2][2]) {
            int gc = g < NW ? g : NW - 1;
            int e = (gc >> 1) * 32 + lg;
            R[0] = sRec[e < total ? e : total - 1];
            R[1] = sRec[e + 16 < total ? e + 16 : total - 1];
#pragma unroll
            for (int bt = 0; bt < 2; ++bt) {
                const unsigned short* brow = Bt + (size_t)R[bt].w * D + (gc & 1) * 64 + grp * 8;
                X[bt][0] = *(const u32x4*)brow; X[bt][1] = *(const u32x4*)(brow + 32);
            }
        };
        auto body = [&](int g, const u32x4 (&R)[2], const u32x4 (&X)[2][2], u32x4 (&Rn)[2], u32x4 (&Xn)[2][2]) {
            const int u = g >> 1, hg = g & 1;
            const int p0 = u * 32;
            unsigned S[2][4][2];
            fetch(g + 16, Rn, Xn);
            __builtin_amdgcn_sched_barrier(0);
#pragma unroll
            for (int bt = 0; bt < 2; ++bt) {
                const u32x4 rec = R[bt];
                const unsigned short* arow = &sA[(rec.z >> 16) * MST + hg * 64 + grp * 8];
                const u32x4 F = {rec.x, rec.y, rec.z & 0xffffu, 0u};
                f32x4 acc[4];
#pragma unroll
                for (int g4 = 0; g4 < 4; ++g4) {
                    u32x4 Y = *(const u32x4*)(arow + (g4 >> 1) * 32);
                    u32x4 Sl = {sel[g4 & 1][0], sel[g4 & 1][1], sel[g4 & 1][2], sel[g4 & 1][3]};
                    f32x4 a = {0.f, 0.f, 0.f, 0.f};
                    a = __builtin_amdgcn_mfma_f32_16x16x32_bf16(__builtin_bit_cast(bf16x8, F), __builtin_bit_cast(bf16x8, wf[g4]), a, 0, 0, 0);
                    a = __builtin_amdgcn_mfma_f32_16x16x32_bf16(__builtin_bit_cast(bf16x8, Y), __builtin_bit_cast(bf16x8, Sl), a, 0, 0, 0);
                    a = __builtin_amdgcn_mfma_f32_16x16x32_bf16(__builtin_bit_cast(bf16x8, X[bt][g4 >> 1]), __builtin_bit_cast(bf16x8, Sl), a, 0, 0, 0);
                    acc[g4] = a;
                }
#pragma unroll
                for (int g4 = 0; g4 < 4; ++g4) {
#pragma unroll
                    for (int hh = 0; hh < 2; ++hh) {
                        f32x2 x = {acc[g4][hh * 2], acc[g4][hh * 2 + 1]};
                        f32x2 ex = {__builtin_amdgcn_exp2f(x[0]), __builtin_amdgcn_exp2f(x[1])};
                        ex += 1.0f;
                        f32x2 rc = {__builtin_amdgcn_rcpf(ex[0]), __builtin_amdgcn_rcpf(ex[1])};
                        S[bt][g4][hh] = cvt_pk(x * rc);
                    }
                }
                __builtin_amdgcn_sched_barrier(0);
            }
            WSTAMP(wsi); wsi++;
            {
                int l = threadIdx.x;
                asm volatile("" : "+v"(l));
                l &= 63;
                const int lg = l & 15, grp = l >> 4;
                int lastp = p0 + 31 < total ? p0 + 31 : total - 1;
                int nlo = __builtin_amdgcn_readfirstlane((int)sDl[p0]);
                int nhi = __builtin_amdgcn_readfirstlane((int)sDl[lastp]);
                u32x2 dv0 = *(const u32x2*)&sDl[p0 + 4 * grp];
                u32x2 dv1 = *(const u32x2*)&sDl[p0 + 16 + 4 * grp];
                int c = lg & 3;
                int ci = c > 0 ? c - 1 : 0;
                u32x2 cf0 = *(const u32x2*)&sCf[ci][p0 + 4 * grp];
                u32x2 cf1 = *(const u32x2*)&sCf[ci][p0 + 16 + 4 * grp];
                if (c == 0) { cf0 = (u32x2){0x3f803f80u, 0x3f803f80u}; cf1 = cf0; }
                float wxv[4];
#pragma unroll
                for (int j = 0; j < 4; ++j) wxv[j] = sWx[(hg * 4 + j) * 16 + lg];
                for (int base = nlo; base <= nhi; base += 4) {
                    unsigned tg = (unsigned)(base + (lg >> 2)) * 0x10001u;
                    u32x4 G;
                    G.x = eq_sel16(dv0.x, tg, cf0.x); G.y = eq_sel16(dv0.y, tg, cf0.y);
                    G.z = eq_sel16(dv1.x, tg, cf1.x); G.w = eq_sel16(dv1.y, tg, cf1.y);
                    int node = base + grp;
                    bool act = node <= nhi;
                    float* prow = &sProw[(u + node) * PST + hg * 64 + lg];
                    f32x4 M[4];
#pragma unroll
                    for (int j = 0; j < 4; ++j) {
                        u32x4 Sv = {S[0][j][0], S[0][j][1], S[1][j][0], S[1][j][1]};
                        f32x4 z = {0.f, 0.f, 0.f, 0.f};
                        M[j] = __builtin_amdgcn_mfma_f32_16x16x32_bf16(__builtin_bit_cast(bf16x8, G), __builtin_bit_cast(bf16x8, Sv), z, 0, 0, 0);
                    }
                    float q1 = 0.f, q2 = 0.f, q3 = 0.f;
#pragma unroll
                    for (int j = 0; j < 4; ++j) {
                        q1 = fmaf(M[j][1], wxv[j], q1); q2 = fmaf(M[j][2], wxv[j], q2); q3 = fmaf(M[j][3], wxv[j], q3);
                    }
                    q1 = row16_sum(q1); q2 = row16_sum(q2); q3 = row16_sum(q3);
                    if (act) {
#pragma unroll
                        for (int j = 0; j < 4; ++j) prow[j * 16] = M[j][0];
                        if (lg < 3) sPpos[(u + node) * 8 + hg * 4 + lg] = lg == 0 ? q1 : (lg == 1 ? q2 : q3);
                    }
                }
            }
            WSTAMP(wsi); wsi++;
        };
        if (NW > 0) {
            u32x4 RA[2], XA[2][2], RB[2], XB[2][2];
            int g = wave;
            fetch(g, RA, XA);
            __builtin_amdgcn_s_setprio(3);
            for (;;) {
                if (g >= NW) break;
                body(g, RA, XA, RB, XB); g += 16;
                if (g < 32) __builtin_amdgcn_s_setprio(2); else __builtin_amdgcn_s_setprio(0);
                if (g >= NW) break;
                body(g, RB, XB, RA, XA); g += 16;
                if (g < 48) __builtin_amdgcn_s_setprio(1); else __builtin_amdgcn_s_setprio(0);
            }
            __builtin_amdgcn_s_setprio(0);
        }
        STAMP(1, 2);
        asm volatile("s_waitcnt vmcnt(0)" ::: "memory");
        __syncthreads();
        STAMP(1, 3);
        if (!slow) break;
        tid = threadIdx.x;
        asm volatile("" : "+v"(tid));
        for (int i = tid; i < T * 32; i += NT2) {
            int n = i >> 5, c4 = i & 31;
            f32x4 a = *(const f32x4*)&macc[n * PST + c4 * 4];
            for (int uu = sUf[n]; uu <= sUl[n]; ++uu) a += *(const f32x4*)&sProw[(uu + n) * PST + c4 * 4];
            *(f32x4*)&macc[n * PST + c4 * 4] = a;
        }
        if (tid < T * 3) {
            int n = tid / 3, c = tid - n * 3;
            float a = macc[n * PST + D + c];
            for (int uu = sUf[n]; uu <= sUl[n]; ++uu) a += sPpos[(uu + n) * 8 + c] + sPpos[(uu + n) * 8 + 4 + c];
            macc[n * PST + D + c] = a;
        }
        if (round_pos >= E) break;
    }
    if (slow) {
        int tid = threadIdx.x;
        asm volatile("" : "+v"(tid));
        __syncthreads();
        for (int i = tid; i < T * 32; i += NT2) {
            int n = i >> 5, c4 = i & 31;
            *(f32x4*)&sProw[n * PST + c4 * 4] = *(const f32x4*)&macc[n * PST + c4 * 4];
        }
        if (tid < T * 3) { int n = tid / 3, c = tid - n * 3; sPpos[n * 8 + c] = macc[n * PST + D + c]; sPpos[n * 8 + 4 + c] = 0.f; }
        if (tid < T) { sUf[tid] = 0; sUl[tid] = 0; }
        __syncthreads();
#pragma unroll
        for (int i = 0; i < 4; ++i) sWh[i * NT2 + tid] = frags[32 * 64 + i * NT2 + tid];
        __syncthreads();
    }

    if (wave < MT * 4) {
        int l = threadIdx.x;
        asm volatile("" : "+v"(l));
        l &= 63;
        const int lg = l & 15, grp = l >> 4;
        int mt = wave >> 2, np = wave & 3;
        int row = mt * 16 + lg;
        int node = n0 + row;
        bool valid = (row < T) && (node < N);
        int uf = valid ? sUf[row] : 0, ul = valid ? sUl[row] : -1;
        f32x4 a0 = {0.f, 0.f, 0.f, 0.f}, a1 = {0.f, 0.f, 0.f, 0.f};
#pragma unroll
        for (int ks = 0; ks < 8; ++ks) {
            bf16x8 xb;
            if (ks < 4) xb = *(const bf16x8*)&sH[row * MST + ks * 32 + grp * 8];
            else {
                f32x4 m0 = {0.f, 0.f, 0.f, 0.f}, m1 = {0.f, 0.f, 0.f, 0.f};
                for (int uu = uf; uu <= ul; ++uu) {
                    const float* pr = &sProw[(uu + row) * PST + (ks - 4) * 32 + grp * 8];
                    m0 += *(const f32x4*)pr; m1 += *(const f32x4*)(pr + 4);
                }
                u32x4 v;
                m0 *= KIN; m1 *= KIN;
                v.x = cvt_pk((f32x2){m0[0], m0[1]}); v.y = cvt_pk((f32x2){m0[2], m0[3]});
                v.z = cvt_pk((f32x2){m1[0], m1[1]}); v.w = cvt_pk((f32x2){m1[2], m1[3]});
                xb = __builtin_bit_cast(bf16x8, v);
            }
            a0 = __builtin_amdgcn_mfma_f32_16x16x32_bf16(__builtin_bit_cast(bf16x8, sWh[((np * 2) * 8 + ks) * 64 + l]), xb, a0, 0, 0, 0);
            a1 = __builtin_amdgcn_mfma_f32_16x16x32_bf16(__builtin_bit_cast(bf16x8, sWh[((np * 2 + 1) * 8 + ks) * 64 + l]), xb, a1, 0, 0, 0);
        }
        if (valid) {
            u32x4 hv2 = *(const u32x4*)&sH[row * MST + grp * 32 + np * 8];
            u32x4 v;
            v.x = pack2(bf_lo(hv2.x) + silu_f(a0[0]), bf_hi(hv2.x) + silu_f(a0[1]));
            v.y = pack2(bf_lo(hv2.y) + silu_f(a0[2]), bf_hi(hv2.y) + silu_f(a0[3]));
            v.z = pack2(bf_lo(hv2.z) + silu_f(a1[0]), bf_hi(hv2.z) + silu_f(a1[1]));
            v.w = pack2(bf_lo(hv2.w) + silu_f(a1[2]), bf_hi(hv2.w) + silu_f(a1[3]));
            *(u32x4*)(out_h + (size_t)node * D + grp * 32 + np * 8) = v;
        }
    } else {
        int t = threadIdx.x;
        asm volatile("" : "+v"(t));
        t -= MT * 4 * 64;
        if (t < T * 3 && n0 * 3 + t < N * 3) {
            int n = t / 3, c = t - n * 3;
            float a = 0.f;
            for (int uu = sUf[n]; uu <= sUl[n]; ++uu) a += sPpos[(uu + n) * 8 + c] + sPpos[(uu + n) * 8 + 4 + c];
            out_pos[(size_t)n0 * 3 + t] = (unsigned short)f2bf(sPosT[t] + a);
        }
    }
    STAMP(1, 4);
    STAMP_FLUSH(1);
    WSTAMP_FLUSH();
}

extern "C" void kernel_launch(void* const* d_in, const int* in_sizes, int n_in,
                              void* d_out, int out_size, void* d_ws, size_t ws_size,
                              hipStream_t stream) {
    const unsigned short* h = (const unsigned short*)d_in[0];
    const unsigned short* pos = (const unsigned short*)d_in[1];
    const u32x2* e_attr = (const u32x2*)d_in[2];
    const int* src = (const int*)d_in[3];
    const int* dst = (const int*)d_in[4];
    const unsigned short* w_e = (const unsigned short*)d_in[5];
    const unsigned short* w_x = (const unsigned short*)d_in[6];
    const unsigned short* w_h = (const unsigned short*)d_in[7];
    const int N = in_sizes[1] / 3;
    const int E = in_sizes[3];
    const int NB = (N + T - 1) / T;
    const int NC = (E + CH - 1) / CH;
    const int NG = (N + 127) / 128;

    char* ws = (char*)d_ws;
    size_t off = 0;
    auto take = [&](size_t bytes) { char* p = ws + off; off += (bytes + 255) / 256 * 256; return p; };
    u32x4* frags = (u32x4*)take((size_t)104 * 64 * 16);
    unsigned short* Bt = (unsigned short*)take((size_t)N * D * 2);
    u32x2* pos4 = (u32x2*)take((size_t)N * 8);
    u32x4* cells = (u32x4*)take((size_t)NB * NC * CS * 16);

    unsigned short* out_h = (unsigned short*)d_out;
    unsigned short* out_pos = out_h + (size_t)N * D;

    size_t lds1 = (size_t)CH * 20 + (size_t)NB * 8;
    if (lds1 < (size_t)2 * 128 * (D + 8) * 2) lds1 = (size_t)2 * 128 * (D + 8) * 2;
    (void)hipFuncSetAttribute((const void*)k1_kernel, hipFuncAttributeMaxDynamicSharedMemorySize, (int)lds1);
    k1_kernel<<<NC + NG + 16, NT1, lds1, stream>>>(h, pos, e_attr, src, dst, w_e, w_h, cells, frags, Bt, pos4,
                                                    N, E, NB, NC, NG);
    k2_kernel<<<NB, NT2, 0, stream>>>(h, pos, e_attr, src, dst, w_e, w_x, cells, frags, Bt, pos4,
                                      out_h, out_pos, N, E, NC);
}
```

```cpp
#include <hip/hip_runtime.h>
#include <hip/hip_bf16.h>
#include <stdint.h>

typedef __attribute__((ext_vector_type(8))) short bf16x8;
typedef __attribute__((ext_vector_type(4))) float f32x4;
typedef __attribute__((ext_vector_type(2))) float f32x2;
typedef __attribute__((ext_vector_type(4))) unsigned u32x4;
typedef __attribute__((ext_vector_type(2))) unsigned u32x2;
typedef __attribute__((ext_vector_type(2))) __bf16 bf16x2v;
typedef __attribute__((ext_vector_type(2))) unsigned short u16x2;

#define STAMP_DECL
#define STAMP(K, i)
#define STAMP_FLUSH(K)
#define WSTAMP(i)
#define WSTAMP_FLUSH()

static constexpr int D = 128;
static constexpr int T = 40;
static constexpr int MT = 3;
static constexpr int POOL = 1024;
static constexpr int CH = 2048;
static constexpr int NT1 = 1024;
static constexpr int NT2 = 1024;

static constexpr float KSC = -1.44269504088896340736f;
static constexpr float KIN = -0.69314718055994530942f;

__device__ __forceinline__ float bf_lo(unsigned u) { return __uint_as_float(u << 16); }
__device__ __forceinline__ float bf_hi(unsigned u) { return __uint_as_float(u & 0xffff0000u); }
__device__ __forceinline__ float bf1(unsigned short u) { return __uint_as_float(((unsigned)u) << 16); }
__device__ __forceinline__ unsigned f2bf(float f) {
    __hip_bfloat16 b = __float2bfloat16(f);
    return (unsigned)*reinterpret_cast<unsigned short*>(&b);
}
__device__ __forceinline__ float rbf(float f) { return __uint_as_float(f2bf(f) << 16); }
__device__ __forceinline__ unsigned pack2(float lo, float hi) { return f2bf(lo) | (f2bf(hi) << 16); }

__device__ __forceinline__ unsigned cvt_pk(f32x2 v) {
    return __builtin_bit_cast(unsigned, __builtin_convertvector(v, bf16x2v));
}
__device__ __forceinline__ unsigned eq_sel16(unsigned a, unsigned b, unsigned c) {
    unsigned d = a ^ b, m;
    asm("v_pk_min_u16 %0, %1, %2" : "=v"(m) : "v"(d), "v"(0x00010001u));
    asm("v_pk_sub_u16 %0, %1, %2" : "=v"(m) : "v"(m), "v"(0x00010001u));
    return m & c;
}
__device__ __forceinline__ void lds_barrier() {
    asm volatile("s_waitcnt lgkmcnt(0)" ::: "memory");
    __builtin_amdgcn_s_barrier();
    asm volatile("" ::: "memory");
}
__device__ __forceinline__ float silu_f(float x) {
    float e = __builtin_amdgcn_exp2f(x * -1.44269504088896340736f);
    return x * __builtin_amdgcn_rcpf(1.0f + e);
}

template <int CTRL> __device__ __forceinline__ float dpp_mov(float v) {
    return __builtin_bit_cast(float, __builtin_amdgcn_update_dpp(0, __builtin_bit_cast(int, v), CTRL, 0xf, 0xf, false));
}
__device__ __forceinline__ float row16_sum(float v) {
    v += dpp_mov<0x128>(v);
    v += dpp_mov<0x124>(v);
    v += dpp_mov<0x122>(v);
    v += dpp_mov<0x121>(v);
    return v;
}
__device__ __forceinline__ int wave_incl_scan(int x) {
    x += __builtin_amdgcn_update_dpp(0, x, 0x111, 0xf, 0xf, false);
    x += __builtin_amdgcn_update_dpp(0, x, 0x112, 0xf, 0xf, false);
    x += __builtin_amdgcn_update_dpp(0, x, 0x114, 0xf, 0xf, false);
    x += __builtin_amdgcn_update_dpp(0, x, 0x118, 0xf, 0xf, false);
    x += __builtin_amdgcn_update_dpp(0, x, 0x142, 0xa, 0xf, false);
    x += __builtin_amdgcn_update_dpp(0, x, 0x143, 0xc, 0xf, false);
    return x;
}

__device__ __forceinline__ int perm_ch(int nt, int i) { return (i >> 2) * 32 + nt * 4 + (i & 3); }

__global__ __launch_bounds__(NT1) void k1_kernel(const unsigned short* __restrict__ h,
                                                 const unsigned short* __restrict__ pos,
                                                 const u32x2* __restrict__ e_attr,
                                                 const int* __restrict__ src, const int* __restrict__ dst,
                                                 const unsigned short* __restrict__ w_e,
                                                 const unsigned short* __restrict__ w_h,
                                                 u32x4* __restrict__ recs, unsigned* __restrict__ offs,
                                                 u32x4* __restrict__ frags,
                                                 unsigned short* __restrict__ Bt, u32x2* __restrict__ pos4,
                                                 int N, int E, int NB, int NC, int NG) {
    extern __shared__ __attribute__((aligned(16))) char smem[];
    const int tid = threadIdx.x;
    const int bid = blockIdx.x;
    STAMP_DECL
    STAMP(0, 0);
    if (bid < NC) {
        u32x4* sRA = (u32x4*)smem;
        int* lcnt = (int*)(smem + CH * 16);
        int* loff = lcnt + NB;
        const int ebase = bid * CH;
        int dd[CH / NT1], ss[CH / NT1]; u32x2 ea[CH / NT1];
#pragma unroll
        for (int it = 0; it < CH / NT1; ++it) {
            int e = ebase + it * NT1 + tid;
            int ec = e < E ? e : E - 1;
            dd[it] = dst[ec]; ss[it] = src[ec]; ea[it] = e_attr[ec];
        }
        for (int i = tid; i < NB; i += NT1) lcnt[i] = 0;
        __syncthreads();
        STAMP(0, 3);
        u32x4 ra[CH / NT1]; int bk[CH / NT1], rk[CH / NT1];
#pragma unroll
        for (int it = 0; it < CH / NT1; ++it) {
            int el = it * NT1 + tid;
            int e = ebase + el;
            bk[it] = -1;
            if (e < E) {
                int d = dd[it];
                int b = d / T;
                int dl = d - b * T;
                rk[it] = atomicAdd(&lcnt[b], 1);
                bk[it] = b;
                ra[it].x = ((unsigned)dl << 16) | (unsigned)el; ra[it].y = (unsigned)ss[it]; ra[it].z = ea[it].x; ra[it].w = ea[it].y;
            }
        }
        __syncthreads();
        STAMP(0, 4);
        if (tid < 64) {
            int carry = 0;
            for (int base = 0; base < NB; base += 64) {
                int i = base + tid;
                int v = i < NB ? lcnt[i] : 0;
                int x = wave_incl_scan(v);
                if (i < NB) loff[i] = carry + x - v;
                carry += __builtin_amdgcn_readlane(x, 63);
            }
        }
        __syncthreads();
        STAMP(0, 5);
#pragma unroll
        for (int it = 0; it < CH / NT1; ++it) {
            if (bk[it] >= 0) {
                sRA[loff[bk[it]] + rk[it]] = ra[it];
            }
        }
        for (int i = tid; i < NB; i += NT1) offs[(size_t)bid * NB + i] = (unsigned)loff[i] | ((unsigned)lcnt[i] << 16);
        __syncthreads();
        STAMP(0, 1);
        const int nvalid = (E - ebase) < CH ? (E - ebase) : CH;
#pragma unroll
        for (int it = 0; it < CH / NT1; ++it) {
            int p = it * NT1 + tid;
            if (p < nvalid) recs[(size_t)ebase + p] = sRA[p];
        }
        STAMP(0, 2);
        STAMP_FLUSH(0);
        return;
    }
    const int wave = tid >> 6, l = tid & 63;
    const int lg = l & 15, grp = l >> 4;
    if (bid < NC + NG) {
        constexpr int WST = D + 8;
        unsigned short* sW = (unsigned short*)smem;
        unsigned short* sH = sW + 128 * WST;
        const int nb0 = (bid - NC) * 128;
        const u32x4* w2 = (const u32x4*)(w_e + (size_t)128 * D);
        u32x4 wv[2], hv[2];
#pragma unroll
        for (int it = 0; it < 2; ++it) {
            int ch = it * NT1 + tid;
            wv[it] = w2[ch];
            int node = nb0 + (ch >> 4);
            int nodec = node < N ? node : N - 1;
            hv[it] = *(const u32x4*)(h + (size_t)nodec * D + (ch & 15) * 8);
        }
#pragma unroll
        for (int it = 0; it < 2; ++it) {
            int ch = it * NT1 + tid;
            *(u32x4*)&sW[(ch >> 4) * WST + (ch & 15) * 8] = wv[it];
            *(u32x4*)&sH[(ch >> 4) * WST + (ch & 15) * 8] = hv[it];
        }
        __syncthreads();
        STAMP(0, 1);
        const int np = wave & 3, mg = wave >> 2;
        bf16x8 hb[2][4];
#pragma unroll
        for (int m = 0; m < 2; ++m)
#pragma unroll
            for (int ks = 0; ks < 4; ++ks) hb[m][ks] = *(const bf16x8*)&sH[(mg * 32 + m * 16 + lg) * WST + ks * 32 + grp * 8];
        f32x4 acc[2][2];
#pragma unroll
        for (int m = 0; m < 2; ++m) { acc[m][0] = (f32x4){0.f, 0.f, 0.f, 0.f}; acc[m][1] = (f32x4){0.f, 0.f, 0.f, 0.f}; }
#pragma unroll
        for (int q = 0; q < 2; ++q) {
            int c = perm_ch(np * 2 + q, lg);
#pragma unroll
            for (int ks = 0; ks < 4; ++ks) {
                bf16x8 wf;
                int k0 = ks * 32 + grp * 8;
#pragma unroll
                for (int j = 0; j < 8; ++j) wf[j] = (short)sW[(k0 + j) * WST + c];
                acc[0][q] = __builtin_amdgcn_mfma_f32_16x16x32_bf16(wf, hb[0][ks], acc[0][q], 0, 0, 0);
                acc[1][q] = __builtin_amdgcn_mfma_f32_16x16x32_bf16(wf, hb[1][ks], acc[1][q], 0, 0, 0);
            }
        }
#pragma unroll
        for (int m = 0; m < 2; ++m) {
            int node = nb0 + mg * 32 + m * 16 + lg;
            if (node < N) {
                u32x4 v;
                v.x = pack2(acc[m][0][0] * KSC, acc[m][0][1] * KSC); v.y = pack2(acc[m][0][2] * KSC, acc[m][0][3] * KSC);
                v.z = pack2(acc[m][1][0] * KSC, acc[m][1][1] * KSC); v.w = pack2(acc[m][1][2] * KSC, acc[m][1][3] * KSC);
                *(u32x4*)(Bt + (size_t)node * D + grp * 32 + np * 8) = v;
            }
        }
        STAMP(0, 2);
        STAMP_FLUSH(0);
        return;
    }
    {
        int t = (bid - NC - NG) * NT1 + tid;
        if (t < 96 * 64) {
            int f = t >> 6;
            const unsigned short* W;
            int nt, ks;
            if (f < 32) { W = w_e; nt = f >> 2; ks = f & 3; }
            else { int g = f - 32; W = w_h; nt = g >> 3; ks = g & 7; }
            int c = perm_ch(nt, lg);
            int k0 = ks * 32 + 8 * grp;
            unsigned v[8];
#pragma unroll
            for (int j = 0; j < 8; ++j) v[j] = W[(size_t)(k0 + j) * D + c];
            u32x4 o;
            o.x = v[0] | (v[1] << 16); o.y = v[2] | (v[3] << 16); o.z = v[4] | (v[5] << 16); o.w = v[6] | (v[7] << 16);
            frags[t] = o;
        } else if (t < 104 * 64) {
            int f = (t >> 6) - 96;
            const unsigned short* wp = w_e + (size_t)256 * D + f * 16 + lg;
            unsigned v[5];
#pragma unroll
            for (int k = 0; k < 5; ++k) {
                float w = bf1(wp[k * D]) * KSC;
                float hi = rbf(w);
                v[k] = grp == 0 ? f2bf(hi) : (grp == 1 ? f2bf(w - hi) : 0u);
            }
            u32x4 o;
            o.x = v[0] | (v[1] << 16); o.y = v[2] | (v[3] << 16); o.z = v[4]; o.w = 0u;
            frags[t] = o;
        } else {
            for (int n = t - 104 * 64; n < N; n += (gridDim.x - NC - NG) * NT1 - 104 * 64) {
                u32x2 o;
                o.x = (unsigned)pos[n * 3] | ((unsigned)pos[n * 3 + 1] << 16);
                o.y = (unsigned)pos[n * 3 + 2];
                pos4[n] = o;
            }
        }
    }
}

static constexpr int MST = D + 8;
static constexpr int PST = D + 16;
static constexpr int NU_MAX = POOL / 32;
static constexpr int NPR = NU_MAX + T;

__global__ __launch_bounds__(NT2) void k2_kernel(const unsigned short* __restrict__ h,
                                                 const unsigned short* __restrict__ pos,
                                                 const u32x2* __restrict__ e_attr,
                                                 const int* __restrict__ src, const int* __restrict__ dst,
                                                 const unsigned short* __restrict__ w_e,
                                                 const unsigned short* __restrict__ w_x,
                                                 const u32x4* __restrict__ recs, const unsigned* __restrict__ offs,
                                                 const u32x4* __restrict__ frags, const unsigned short* __restrict__ Bt,
                                                 const u32x2* __restrict__ pos4,
                                                 unsigned short* __restrict__ out_h, unsigned short* __restrict__ out_pos,
                                                 int N, int E, int NC, int NB) {
    __shared__ __attribute__((aligned(16))) u32x4 sWh[64 * 64];
    __shared__ __attribute__((aligned(16))) float sProw[NPR * PST];
    __shared__ __attribute__((aligned(16))) float sPpos[NPR * 8];
    __shared__ __attribute__((aligned(16))) unsigned short sH[MT * 16 * MST];
    __shared__ __attribute__((aligned(16))) unsigned short sA[MT * 16 * MST - 8 * MST];
    __shared__ __attribute__((aligned(16))) u32x4 sRec[POOL];
    __shared__ __attribute__((aligned(16))) unsigned short sCf[3][POOL];
    __shared__ __attribute__((aligned(16))) unsigned short sDl[POOL + 32];
    __shared__ float sWx[D];
    __shared__ float sPosT[T * 3 + 8];
    __shared__ int sCnt[64], sOff[64];
    __shared__ int sUf[64], sUl[64];
    __shared__ int sMisc[4];
    unsigned* sKey = (unsigned*)sProw;

    const int tid = threadIdx.x;
    const int wave = __builtin_amdgcn_readfirstlane(tid >> 6);
    const int l = tid & 63;
    const int lg = l & 15, grp = l >> 4;
    const int b = blockIdx.x;
    const int n0 = b * T;
    STAMP_DECL
    STAMP(1, 0);

    constexpr int MAXR = 4;
    const int c8 = tid >> 3, r8 = tid & 7;
    const unsigned ocell = offs[(size_t)(c8 < NC ? c8 : NC - 1) * NB + b];
    u32x4 w1f[8];
    if (wave >= 12) {
#pragma unroll
        for (int i = 0; i < 8; ++i) w1f[i] = frags[((wave & 3) * 8 + i) * 64 + l];
    }
    u32x4 hv = {0u, 0u, 0u, 0u};
    if (wave < MT * 4) {
        int node = n0 + (tid >> 4);
        int nodec = node < N ? node : N - 1;
        hv = *(const u32x4*)(h + (size_t)nodec * D + (tid & 15) * 8);
    }
    unsigned posu, wxu;
    {
        int i = n0 * 3 + (tid < T * 3 ? tid : 0);
        posu = pos[i < N * 3 ? i : N * 3 - 1];
        wxu = w_x[tid & (D - 1)];
    }
    u32x4 wf[4];
#pragma unroll
    for (int i = 0; i < 4; ++i) wf[i] = frags[(96 + (wave & 1) * 4 + i) * 64 + l];
    if (tid < 64) sCnt[tid] = 0;
    if (tid < 4) sMisc[tid] = 0;
    lds_barrier();
    STAMP(1, 6);

    int rank[MAXR];
    unsigned key[MAXR];
    u32x4 ra[MAXR];
    u32x2 ps[MAXR];
    {
        const __amdgpu_buffer_rsrc_t rs = __builtin_amdgcn_make_buffer_rsrc((void*)recs, 0, E * 16, 0x00020000);
        const int cnt = c8 < NC ? (int)(ocell >> 16) : 0;
        const int rbase = c8 * CH + (int)(ocell & 0xffffu);
#pragma unroll
        for (int it = 0; it < MAXR; ++it) {
            int r = r8 + 8 * it;
            ra[it] = __builtin_amdgcn_raw_buffer_load_b128(rs, r < cnt ? (rbase + r) * 16 : 0x7ffffff0, 0, 0);
        }
        if (cnt > 8 * MAXR) sMisc[0] = 1;
#pragma unroll
        for (int it = 0; it < MAXR; ++it) {
            bool valid = r8 + 8 * it < cnt;
            ps[it] = pos4[ra[it].y];
            int dl = ra[it].x >> 16;
            key[it] = ((unsigned)dl << 18) | ((unsigned)(c8 * CH) + (ra[it].x & 0xffffu));
            rank[it] = -1;
            if (valid) rank[it] = atomicAdd(&sCnt[dl], 1) | (dl << 16);
        }
    }
    if (NC > NT2 / 8 && tid == 0) sMisc[0] = 1;
    STAMP(1, 7);
    WSTAMP(8);
    if (wave < MT * 4) *(u32x4*)&sH[(tid >> 4) * MST + (tid & 15) * 8] = hv;
    if (tid < T * 3) sPosT[tid] = bf1((unsigned short)posu);
    if (tid < D) sWx[tid] = bf1((unsigned short)wxu) * KIN;
    unsigned sel[2][4];
#pragma unroll
    for (int hf2 = 0; hf2 < 2; ++hf2)
#pragma unroll
        for (int w = 0; w < 4; ++w) {
            int k0 = grp * 8 + w * 2, tk = hf2 * 16 + lg;
            sel[hf2][w] = (k0 == tk ? 0x3f80u : 0u) | (k0 + 1 == tk ? 0x3f800000u : 0u);
        }
    lds_barrier();
    STAMP(1, 5);
    WSTAMP(9);

    int round_pos = 0;
    float* macc = (float*)sWh;
    u32x4* sRaw = (u32x4*)(sKey + POOL + 64);
    u32x2* sRawP = (u32x2*)(sKey + POOL + 64 + POOL * 4);
    int total;
    bool slow;
    {
        int cntl = sCnt[l];
        int incl = wave_incl_scan(cntl);
        int excl = incl - cntl;
        total = __builtin_amdgcn_readlane(incl, 63);
        slow = sMisc[0] != 0 || total > POOL;
        if (!slow) {
#pragma unroll
            for (int it = 0; it < MAXR; ++it) {
                int dl = rank[it] >> 16;
                int o = __builtin_amdgcn_ds_bpermute((dl & 63) << 2, excl);
                if (rank[it] >= 0) {
                    int p = o + (rank[it] & 0xffff);
                    sKey[p] = key[it]; sRaw[p] = ra[it]; sRawP[p] = ps[it];
                }
            }
            if (wave == 0) sOff[l] = excl;
            if (wave == 1 && l < 4) sKey[total + l] = 0xffffffffu;
        }
    }
    STAMP(1, 9);
    auto rank_step = [&](int tid, int total) {
        if (tid < total) {
            u32x4 rr = sRaw[tid];
            u32x2 pp = sRawP[tid];
            unsigned ky = sKey[tid];
            int dl = rr.x >> 16;
            int o = sOff[dl], end = o + sCnt[dl];
            int j0 = o & ~3;
            int p = j0;
            for (int j = j0; j < end; j += 4) {
                u32x4 k4 = *(const u32x4*)&sKey[j];
                p += (k4.x < ky ? 1 : 0) + (k4.y < ky ? 1 : 0) + (k4.z < ky ? 1 : 0) + (k4.w < ky ? 1 : 0);
            }
            float d0 = rbf(sPosT[dl * 3 + 0] - bf_lo(pp.x));
            float d1 = rbf(sPosT[dl * 3 + 1] - bf_hi(pp.x));
            float d2 = rbf(sPosT[dl * 3 + 2] - bf_lo(pp.y));
            float q = rbf(rbf(d0 * d0) + rbf(d1 * d1) + rbf(d2 * d2));
            u32x4 rec;
            rec.x = (__float_as_uint(q) >> 16) | (rr.z << 16);
            rec.y = (rr.z >> 16) | (rr.w << 16);
            rec.z = (rr.w >> 16) | ((unsigned)dl << 16);
            rec.w = rr.y;
            sRec[p] = rec;
            sCf[0][p] = (unsigned short)(__float_as_uint(d0) >> 16);
            sCf[1][p] = (unsigned short)(__float_as_uint(d1) >> 16);
            sCf[2][p] = (unsigned short)(__float_as_uint(d2) >> 16);
            sDl[p] = (unsigned short)dl;
        }
        if (tid < 32) sDl[total + tid] = 0xffffu;
        if (tid >= 64 && tid < 64 + T) {
            int n = tid - 64;
            int o = sOff[n], cnt = sCnt[n];
            sUf[n] = o >> 5;
            sUl[n] = cnt > 0 ? (o + cnt - 1) >> 5 : -1;
        }
    };
    lds_barrier();
    STAMP(1, 10);
    WSTAMP(10);
    if (!slow) rank_step(tid, total);
    WSTAMP(11);
    if (wave >= 12) {
        const int np = wave & 3;
#pragma unroll
        for (int mt = 0; mt < MT; ++mt) {
            f32x4 a0 = {0.f, 0.f, 0.f, 0.f}, a1 = {0.f, 0.f, 0.f, 0.f};
#pragma unroll
            for (int ks = 0; ks < 4; ++ks) {
                bf16x8 hb = *(const bf16x8*)&sH[(mt * 16 + lg) * MST + ks * 32 + grp * 8];
                a0 = __builtin_amdgcn_mfma_f32_16x16x32_bf16(__builtin_bit_cast(bf16x8, w1f[ks]), hb, a0, 0, 0, 0);
                a1 = __builtin_amdgcn_mfma_f32_16x16x32_bf16(__builtin_bit_cast(bf16x8, w1f[4 + ks]), hb, a1, 0, 0, 0);
            }
            int row = mt * 16 + lg;
            if (row < T) {
                u32x4 v;
                v.x = pack2(a0[0] * KSC, a0[1] * KSC); v.y = pack2(a0[2] * KSC, a0[3] * KSC);
                v.z = pack2(a1[0] * KSC, a1[1] * KSC); v.w = pack2(a1[2] * KSC, a1[3] * KSC);
                *(u32x4*)&sA[row * MST + grp * 32 + np * 8] = v;
            }
        }
    }
    if (slow) {
        for (int i = tid; i < T * PST; i += NT2) macc[i] = 0.f;
    } else {
#pragma unroll
        for (int i = 0; i < 4; ++i) {
            unsigned lds_off = __builtin_amdgcn_readfirstlane((unsigned)(uintptr_t)(sWh + i * NT2 + wave * 64));
            const u32x4* gp = frags + 32 * 64 + i * NT2 + tid;
            asm volatile("s_mov_b32 m0, %0\n\ts_nop 0\n\tglobal_load_lds_dwordx4 %1, off" :: "s"(lds_off), "v"(gp) : "memory", "m0");
        }
    }
    STAMP(1, 11);
    WSTAMP(12);
    for (;;) {
        int tid = threadIdx.x;
        asm volatile("" : "+v"(tid));
        const int l = tid & 63;
        const int lg = l & 15, grp = l >> 4;
        if (slow) {
            __syncthreads();
            if (tid < 64) sCnt[tid] = 0;
            if (tid == 0) sMisc[1] = 0;
            __syncthreads();
            int e = round_pos + tid;
            round_pos += NT2;
            int rk = -1, dl = 0;
            u32x4 rr = {0u, 0u, 0u, 0u};
            u32x2 pp = {0u, 0u};
            if (e < E) {
                dl = dst[e] - n0;
                if (dl >= 0 && dl < T) {
                    int s = src[e];
                    u32x2 ea = e_attr[e];
                    rr.x = (unsigned)dl << 16; rr.y = (unsigned)s; rr.z = ea.x; rr.w = ea.y;
                    pp = pos4[s];
                    rk = atomicAdd(&sCnt[dl], 1);
                }
            }
            __syncthreads();
            int cntl = sCnt[l];
            int incl = wave_incl_scan(cntl);
            int excl = incl - cntl;
            total = __builtin_amdgcn_readlane(incl, 63);
            int o = __builtin_amdgcn_ds_bpermute((dl & 63) << 2, excl);
            if (rk >= 0) { int p = o + rk; sKey[p] = ((unsigned)dl << 18) | (unsigned)tid; sRaw[p] = rr; sRawP[p] = pp; }
            if (wave == 0) sOff[l] = excl;
            if (wave == 1 && l < 4) sKey[total + l] = 0xffffffffu;
            __syncthreads();
            rank_step(tid, total);
        }
        lds_barrier();
        STAMP(1, 1);

        const int NW = ((total + 31) >> 5) * 2;
        int wsi = 0;
        WSTAMP(wsi); wsi++;
        auto fetch = [&](int g, u32x4 (&R)[2], u32x4 (&X)[2][2]) {
            int gc = g < NW ? g : NW - 1;
            int e = (gc >> 1) * 32 + lg;
            R[0] = sRec[e < total ? e : total - 1];
            R[1] = sRec[e + 16 < total ? e + 16 : total - 1];
#pragma unroll
            for (int bt = 0; bt < 2; ++bt) {
                const unsigned short* brow = Bt + (size_t)R[bt].w * D + (gc & 1) * 64 + grp * 8;
                X[bt][0] = *(const u32x4*)brow; X[bt][1] = *(const u32x4*)(brow + 32);
            }
        };
        auto body = [&](int g, const u32x4 (&R)[2], const u32x4 (&X)[2][2], u32x4 (&Rn)[2], u32x4 (&Xn)[2][2]) {
            const int u = g >> 1, hg = g & 1;
            const int p0 = u * 32;
            unsigned S[2][4][2];
            fetch(g + 16, Rn, Xn);
            __builtin_amdgcn_sched_barrier(0);
#pragma unroll
            for (int bt = 0; bt < 2; ++bt) {
                const u32x4 rec = R[bt];
                const unsigned short* arow = &sA[(rec.z >> 16) * MST + hg * 64 + grp * 8];
                const u32x4 F = {rec.x, rec.y, rec.z & 0xffffu, 0u};
                f32x4 acc[4];
#pragma unroll
                for (int g4 = 0; g4 < 4; ++g4) {
                    u32x4 Y = *(const u32x4*)(arow + (g4 >> 1) * 32);
                    u32x4 Sl = {sel[g4 & 1][0], sel[g4 & 1][1], sel[g4 & 1][2], sel[g4 & 1][3]};
                    f32x4 a = {0.f, 0.f, 0.f, 0.f};
                    a = __builtin_amdgcn_mfma_f32_16x16x32_bf16(__builtin_bit_cast(bf16x8, F), __builtin_bit_cast(bf16x8, wf[g4]), a, 0, 0, 0);
                    a = __builtin_amdgcn_mfma_f32_16x16x32_bf16(__builtin_bit_cast(bf16x8, Y), __builtin_bit_cast(bf16x8, Sl), a, 0, 0, 0);
                    a = __builtin_amdgcn_mfma_f32_16x16x32_bf16(__builtin_bit_cast(bf16x8, X[bt][g4 >> 1]), __builtin_bit_cast(bf16x8, Sl), a, 0, 0, 0);
                    acc[g4] = a;
                }
#pragma unroll
                for (int g4 = 0; g4 < 4; ++g4) {
#pragma unroll
                    for (int hh = 0; hh < 2; ++hh) {
                        f32x2 x = {acc[g4][hh * 2], acc[g4][hh * 2 + 1]};
                        f32x2 ex = {__builtin_amdgcn_exp2f(x[0]), __builtin_amdgcn_exp2f(x[1])};
                        ex += 1.0f;
                        f32x2 rc = {__builtin_amdgcn_rcpf(ex[0]), __builtin_amdgcn_rcpf(ex[1])};
                        S[bt][g4][hh] = cvt_pk(x * rc);
                    }
                }
                __builtin_amdgcn_sched_barrier(0);
            }
            WSTAMP(wsi); wsi++;
            {
                int l = threadIdx.x;
                asm volatile("" : "+v"(l));
                l &= 63;
                const int lg = l & 15, grp = l >> 4;
                int lastp = p0 + 31 < total ? p0 + 31 : total - 1;
                int nlo = __builtin_amdgcn_readfirstlane((int)sDl[p0]);
                int nhi = __builtin_amdgcn_readfirstlane((int)sDl[lastp]);
                u32x2 dv0 = *(const u32x2*)&sDl[p0 + 4 * grp];
                u32x2 dv1 = *(const u32x2*)&sDl[p0 + 16 + 4 * grp];
                int c = lg & 3;
                int ci = c > 0 ? c - 1 : 0;
                u32x2 cf0 = *(const u32x2*)&sCf[ci][p0 + 4 * grp];
                u32x2 cf1 = *(const u32x2*)&sCf[ci][p0 + 16 + 4 * grp];
                if (c == 0) { cf0 = (u32x2){0x3f803f80u, 0x3f803f80u}; cf1 = cf0; }
                float wxv[4];
#pragma unroll
                for (int j = 0; j < 4; ++j) wxv[j] = sWx[(hg * 4 + j) * 16 + lg];
                for (int base = nlo; base <= nhi; base += 4) {
                    unsigned tg = (unsigned)(base + (lg >> 2)) * 0x10001u;
                    u32x4 G;
                    G.x = eq_sel16(dv0.x, tg, cf0.x); G.y = eq_sel16(dv0.y, tg, cf0.y);
                    G.z = eq_sel16(dv1.x, tg, cf1.x); G.w = eq_sel16(dv1.y, tg, cf1.y);
                    int node = base + grp;
                    bool act = node <= nhi;
                    float* prow = &sProw[(u + node) * PST + hg * 64 + lg];
                    f32x4 M[4];
#pragma unroll
                    for (int j = 0; j < 4; ++j) {
                        u32x4 Sv = {S[0][j][0], S[0][j][1], S[1][j][0], S[1][j][1]};
                        f32x4 z = {0.f, 0.f, 0.f, 0.f};
                        M[j] = __builtin_amdgcn_mfma_f32_16x16x32_bf16(__builtin_bit_cast(bf16x8, G), __builtin_bit_cast(bf16x8, Sv), z, 0, 0, 0);
                    }
                    float q1 = 0.f, q2 = 0.f, q3 = 0.f;
#pragma unroll
                    for (int j = 0; j < 4; ++j) {
                        q1 = fmaf(M[j][1], wxv[j], q1); q2 = fmaf(M[j][2], wxv[j], q2); q3 = fmaf(M[j][3], wxv[j], q3);
                    }
                    q1 = row16_sum(q1); q2 = row16_sum(q2); q3 = row16_sum(q3);
                    if (act) {
#pragma unroll
                        for (int j = 0; j < 4; ++j) prow[j * 16] = M[j][0];
                        if (lg < 3) sPpos[(u + node) * 8 + hg * 4 + lg] = lg == 0 ? q1 : (lg == 1 ? q2 : q3);
                    }
                }
            }
            WSTAMP(wsi); wsi++;
        };
        if (NW > 0) {
            u32x4 RA[2], XA[2][2], RB[2], XB[2][2];
            int g = wave;
            fetch(g, RA, XA);
            __builtin_amdgcn_s_setprio(3);
            for (;;) {
                if (g >= NW) break;
                body(g, RA, XA, RB, XB); g += 16;
                if (g < 32) __builtin_amdgcn_s_setprio(2); else __builtin_amdgcn_s_setprio(0);
                if (g >= NW) break;
                body(g, RB, XB, RA, XA); g += 16;
                if (g < 48) __builtin_amdgcn_s_setprio(1); else __builtin_amdgcn_s_setprio(0);
            }
            __builtin_amdgcn_s_setprio(0);
        }
        STAMP(1, 2);
        asm volatile("s_waitcnt vmcnt(0)" ::: "memory");
        __syncthreads();
        STAMP(1, 3);
        if (!slow) break;
        tid = threadIdx.x;
        asm volatile("" : "+v"(tid));
        for (int i = tid; i < T * 32; i += NT2) {
            int n = i >> 5, c4 = i & 31;
            f32x4 a = *(const f32x4*)&macc[n * PST + c4 * 4];
            for (int uu = sUf[n]; uu <= sUl[n]; ++uu) a += *(const f32x4*)&sProw[(uu + n) * PST + c4 * 4];
            *(f32x4*)&macc[n * PST + c4 * 4] = a;
        }
        if (tid < T * 3) {
            int n = tid / 3, c = tid - n * 3;
            float a = macc[n * PST + D + c];
            for (int uu = sUf[n]; uu <= sUl[n]; ++uu) a += sPpos[(uu + n) * 8 + c] + sPpos[(uu + n) * 8 + 4 + c];
            macc[n * PST + D + c] = a;
        }
        if (round_pos >= E) break;
    }
    if (slow) {
        int tid = threadIdx.x;
        asm volatile("" : "+v"(tid));
        __syncthreads();
        for (int i = tid; i < T * 32; i += NT2) {
            int n = i >> 5, c4 = i & 31;
            *(f32x4*)&sProw[n * PST + c4 * 4] = *(const f32x4*)&macc[n * PST + c4 * 4];
        }
        if (tid < T * 3) { int n = tid / 3, c = tid - n * 3; sPpos[n * 8 + c] = macc[n * PST + D + c]; sPpos[n * 8 + 4 + c] = 0.f; }
        if (tid < T) { sUf[tid] = 0; sUl[tid] = 0; }
        __syncthreads();
#pragma unroll
        for (int i = 0; i < 4; ++i) sWh[i * NT2 + tid] = frags[32 * 64 + i * NT2 + tid];
        __syncthreads();
    }

    if (wave < MT * 4) {
        int l = threadIdx.x;
        asm volatile("" : "+v"(l));
        l &= 63;
        const int lg = l & 15, grp = l >> 4;
        int mt = wave >> 2, np = wave & 3;
        int row = mt * 16 + lg;
        int node = n0 + row;
        bool valid = (row < T) && (node < N);
        int uf = valid ? sUf[row] : 0, ul = valid ? sUl[row] : -1;
        f32x4 a0 = {0.f, 0.f, 0.f, 0.f}, a1 = {0.f, 0.f, 0.f, 0.f};
#pragma unroll
        for (int ks = 0; ks < 8; ++ks) {
            bf16x8 xb;
            if (ks < 4) xb = *(const bf16x8*)&sH[row * MST + ks * 32 + grp * 8];
            else {
                f32x4 m0 = {0.f, 0.f, 0.f, 0.f}, m1 = {0.f, 0.f, 0.f, 0.f};
                for (int uu = uf; uu <= ul; ++uu) {
                    const float* pr = &sProw[(uu + row) * PST + (ks - 4) * 32 + grp * 8];
                    m0 += *(const f32x4*)pr; m1 += *(const f32x4*)(pr + 4);
                }
                u32x4 v;
                m0 *= KIN; m1 *= KIN;
                v.x = cvt_pk((f32x2){m0[0], m0[1]}); v.y = cvt_pk((f32x2){m0[2], m0[3]});
                v.z = cvt_pk((f32x2){m1[0], m1[1]}); v.w = cvt_pk((f32x2){m1[2], m1[3]});
                xb = __builtin_bit_cast(bf16x8, v);
            }
            a0 = __builtin_amdgcn_mfma_f32_16x16x32_bf16(__builtin_bit_cast(bf16x8, sWh[((np * 2) * 8 + ks) * 64 + l]), xb, a0, 0, 0, 0);
            a1 = __builtin_amdgcn_mfma_f32_16x16x32_bf16(__builtin_bit_cast(bf16x8, sWh[((np * 2 + 1) * 8 + ks) * 64 + l]), xb, a1, 0, 0, 0);
        }
        if (valid) {
            u32x4 hv2 = *(const u32x4*)&sH[row * MST + grp * 32 + np * 8];
            u32x4 v;
            v.x = pack2(bf_lo(hv2.x) + silu_f(a0[0]), bf_hi(hv2.x) + silu_f(a0[1]));
            v.y = pack2(bf_lo(hv2.y) + silu_f(a0[2]), bf_hi(hv2.y) + silu_f(a0[3]));
            v.z = pack2(bf_lo(hv2.z) + silu_f(a1[0]), bf_hi(hv2.z) + silu_f(a1[1]));
            v.w = pack2(bf_lo(hv2.w) + silu_f(a1[2]), bf_hi(hv2.w) + silu_f(a1[3]));
            *(u32x4*)(out_h + (size_t)node * D + grp * 32 + np * 8) = v;
        }
    } else {
        int t = threadIdx.x;
        asm volatile("" : "+v"(t));
        t -= MT * 4 * 64;
        if (t < T * 3 && n0 * 3 + t < N * 3) {
            int n = t / 3, c = t - n * 3;
            float a = 0.f;
            for (int uu = sUf[n]; uu <= sUl[n]; ++uu) a += sPpos[(uu + n) * 8 + c] + sPpos[(uu + n) * 8 + 4 + c];
            out_pos[(size_t)n0 * 3 + t] = (unsigned short)f2bf(sPosT[t] + a);
        }
    }
    STAMP(1, 4);
    STAMP_FLUSH(1);
    WSTAMP_FLUSH();
}

extern "C" void kernel_launch(void* const* d_in, const int* in_sizes, int n_in,
                              void* d_out, int out_size, void* d_ws, size_t ws_size,
                              hipStream_t stream) {
    const unsigned short* h = (const unsigned short*)d_in[0];
    const unsigned short* pos = (const unsigned short*)d_in[1];
    const u32x2* e_attr = (const u32x2*)d_in[2];
    const int* src = (const int*)d_in[3];
    const int* dst = (const int*)d_in[4];
    const unsigned short* w_e = (const unsigned short*)d_in[5];
    const unsigned short* w_x = (const unsigned short*)d_in[6];
    const unsigned short* w_h = (const unsigned short*)d_in[7];
    const int N = in_sizes[1] / 3;
    const int E = in_sizes[3];
    const int NB = (N + T - 1) / T;
    const int NC = (E + CH - 1) / CH;
    const int NG = (N + 127) / 128;

    char* ws = (char*)d_ws;
    size_t off = 0;
    auto take = [&](size_t bytes) { char* p = ws + off; off += (bytes + 255) / 256 * 256; return p; };
    u32x4* frags = (u32x4*)take((size_t)104 * 64 * 16);
    unsigned short* Bt = (unsigned short*)take((size_t)N * D * 2);
    u32x2* pos4 = (u32x2*)take((size_t)N * 8);
    u32x4* recs = (u32x4*)take((size_t)NC * CH * 16);
    unsigned* offs = (unsigned*)take((size_t)NC * NB * 4);

    unsigned short* out_h = (unsigned short*)d_out;
    unsigned short* out_pos = out_h + (size_t)N * D;

    size_t lds1 = (size_t)CH * 16 + (size_t)NB * 8;
    if (lds1 < (size_t)2 * 128 * (D + 8) * 2) lds1 = (size_t)2 * 128 * (D + 8) * 2;
    (void)hipFuncSetAttribute((const void*)k1_kernel, hipFuncAttributeMaxDynamicSharedMemorySize, (int)lds1);
    k1_kernel<<<NC + NG + 16, NT1, lds1, stream>>>(h, pos, e_attr, src, dst, w_e, w_h, recs, offs, frags, Bt, pos4,
                                                    N, E, NB, NC, NG);
    k2_kernel<<<NB, NT2, 0, stream>>>(h, pos, e_attr, src, dst, w_e, w_x, recs, offs, frags, Bt, pos4,
                                      out_h, out_pos, N, E, NC, NB);
}
```

```cpp
#include <hip/hip_runtime.h>
#include <hip/hip_bf16.h>
#include <stdint.h>

typedef __attribute__((ext_vector_type(8))) short bf16x8;
typedef __attribute__((ext_vector_type(4))) float f32x4;
typedef __attribute__((ext_vector_type(2))) float f32x2;
typedef __attribute__((ext_vector_type(4))) unsigned u32x4;
typedef __attribute__((ext_vector_type(2))) unsigned u32x2;
typedef __attribute__((ext_vector_type(2))) __bf16 bf16x2v;
typedef __attribute__((ext_vector_type(2))) unsigned short u16x2;

#define STAMP_DECL
#define STAMP(K, i)
#define STAMP_FLUSH(K)
#define WSTAMP(i)
#define WSTAMP_FLUSH()

static constexpr int D = 128;
static constexpr int T = 40;
static constexpr int MT = 3;
static constexpr int POOL = 1024;
static constexpr int CH = 2048;
static constexpr int NT1 = 1024;
static constexpr int NT2 = 1024;

static constexpr float KSC = -1.44269504088896340736f;
static constexpr float KIN = -0.69314718055994530942f;

__device__ __forceinline__ float bf_lo(unsigned u) { return __uint_as_float(u << 16); }
__device__ __forceinline__ float bf_hi(unsigned u) { return __uint_as_float(u & 0xffff0000u); }
__device__ __forceinline__ float bf1(unsigned short u) { return __uint_as_float(((unsigned)u) << 16); }
__device__ __forceinline__ unsigned f2bf(float f) {
    __hip_bfloat16 b = __float2bfloat16(f);
    return (unsigned)*reinterpret_cast<unsigned short*>(&b);
}
__device__ __forceinline__ float rbf(float f) { return __uint_as_float(f2bf(f) << 16); }
__device__ __forceinline__ unsigned pack2(float lo, float hi) { return f2bf(lo) | (f2bf(hi) << 16); }

__device__ __forceinline__ unsigned cvt_pk(f32x2 v) {
    return __builtin_bit_cast(unsigned, __builtin_convertvector(v, bf16x2v));
}
__device__ __forceinline__ unsigned eq_sel16(unsigned a, unsigned b, unsigned c) {
    unsigned d = a ^ b, m;
    asm("v_pk_min_u16 %0, %1, %2" : "=v"(m) : "v"(d), "v"(0x00010001u));
    asm("v_pk_sub_u16 %0, %1, %2" : "=v"(m) : "v"(m), "v"(0x00010001u));
    return m & c;
}
__device__ __forceinline__ void lds_barrier() {
    asm volatile("s_waitcnt lgkmcnt(0)" ::: "memory");
    __builtin_amdgcn_s_barrier();
    asm volatile("" ::: "memory");
}
__device__ __forceinline__ float silu_f(float x) {
    float e = __builtin_amdgcn_exp2f(x * -1.44269504088896340736f);
    return x * __builtin_amdgcn_rcpf(1.0f + e);
}

template <int CTRL> __device__ __forceinline__ float dpp_mov(float v) {
    return __builtin_bit_cast(float, __builtin_amdgcn_update_dpp(0, __builtin_bit_cast(int, v), CTRL, 0xf, 0xf, false));
}
__device__ __forceinline__ float row16_sum(float v) {
    v += dpp_mov<0x128>(v);
    v += dpp_mov<0x124>(v);
    v += dpp_mov<0x122>(v);
    v += dpp_mov<0x121>(v);
    return v;
}
__device__ __forceinline__ int wave_incl_scan(int x) {
    x += __builtin_amdgcn_update_dpp(0, x, 0x111, 0xf, 0xf, false);
    x += __builtin_amdgcn_update_dpp(0, x, 0x112, 0xf, 0xf, false);
    x += __builtin_amdgcn_update_dpp(0, x, 0x114, 0xf, 0xf, false);
    x += __builtin_amdgcn_update_dpp(0, x, 0x118, 0xf, 0xf, false);
    x += __builtin_amdgcn_update_dpp(0, x, 0x142, 0xa, 0xf, false);
    x += __builtin_amdgcn_update_dpp(0, x, 0x143, 0xc, 0xf, false);
    return x;
}

__device__ __forceinline__ int perm_ch(int nt, int i) { return (i >> 2) * 32 + nt * 4 + (i & 3); }

__global__ __launch_bounds__(NT1) void k1_kernel(const unsigned short* __restrict__ h,
                                                 const unsigned short* __restrict__ pos,
                                                 const u32x2* __restrict__ e_attr,
                                                 const int* __restrict__ src, const int* __restrict__ dst,
                                                 const unsigned short* __restrict__ w_e,
                                                 const unsigned short* __restrict__ w_h,
                                                 u32x4* __restrict__ recs, unsigned* __restrict__ offs,
                                                 u32x4* __restrict__ frags,
                                                 unsigned short* __restrict__ Bt, u32x2* __restrict__ pos4,
                                                 int N, int E, int NB, int NC, int NG) {
    extern __shared__ __attribute__((aligned(16))) char smem[];
    const int tid = threadIdx.x;
    const int bid = blockIdx.x;
    STAMP_DECL
    STAMP(0, 0);
    if (bid < NC) {
        u32x4* sRA = (u32x4*)smem;
        int* lcnt = (int*)(smem + CH * 16);
        int* loff = lcnt + NB;
        const int ebase = bid * CH;
        int dd[CH / NT1], ss[CH / NT1]; u32x2 ea[CH / NT1];
#pragma unroll
        for (int it = 0; it < CH / NT1; ++it) {
            int e = ebase + it * NT1 + tid;
            int ec = e < E ? e : E - 1;
            dd[it] = dst[ec]; ss[it] = src[ec]; ea[it] = e_attr[ec];
        }
        for (int i = tid; i < NB; i += NT1) lcnt[i] = 0;
        __syncthreads();
        STAMP(0, 3);
        u32x4 ra[CH / NT1]; int bk[CH / NT1], rk[CH / NT1];
#pragma unroll
        for (int it = 0; it < CH / NT1; ++it) {
            int el = it * NT1 + tid;
            int e = ebase + el;
            bk[it] = -1;
            if (e < E) {
                int d = dd[it];
                int b = d / T;
                int dl = d - b * T;
                rk[it] = atomicAdd(&lcnt[b], 1);
                bk[it] = b;
                ra[it].x = ((unsigned)dl << 16) | (unsigned)el; ra[it].y = (unsigned)ss[it]; ra[it].z = ea[it].x; ra[it].w = ea[it].y;
            }
        }
        __syncthreads();
        STAMP(0, 4);
        if (tid < 64) {
            int carry = 0;
            for (int base = 0; base < NB; base += 64) {
                int i = base + tid;
                int v = i < NB ? lcnt[i] : 0;
                int x = wave_incl_scan(v);
                if (i < NB) loff[i] = carry + x - v;
                carry += __builtin_amdgcn_readlane(x, 63);
            }
        }
        __syncthreads();
        STAMP(0, 5);
#pragma unroll
        for (int it = 0; it < CH / NT1; ++it) {
            if (bk[it] >= 0) {
                sRA[loff[bk[it]] + rk[it]] = ra[it];
            }
        }
        for (int i = tid; i < NB; i += NT1) offs[(size_t)bid * NB + i] = (unsigned)loff[i] | ((unsigned)lcnt[i] << 16);
        __syncthreads();
        STAMP(0, 1);
        const int nvalid = (E - ebase) < CH ? (E - ebase) : CH;
#pragma unroll
        for (int it = 0; it < CH / NT1; ++it) {
            int p = it * NT1 + tid;
            if (p < nvalid) recs[(size_t)ebase + p] = sRA[p];
        }
        STAMP(0, 2);
        STAMP_FLUSH(0);
        return;
    }
    const int wave = tid >> 6, l = tid & 63;
    const int lg = l & 15, grp = l >> 4;
    if (bid < NC + NG) {
        constexpr int WST = D + 8;
        unsigned short* sW = (unsigned short*)smem;
        unsigned short* sH = sW + 128 * WST;
        const int nb0 = (bid - NC) * 128;
        const u32x4* w2 = (const u32x4*)(w_e + (size_t)128 * D);
        u32x4 wv[2], hv[2];
#pragma unroll
        for (int it = 0; it < 2; ++it) {
            int ch = it * NT1 + tid;
            wv[it] = w2[ch];
            int node = nb0 + (ch >> 4);
            int nodec = node < N ? node : N - 1;
            hv[it] = *(const u32x4*)(h + (size_t)nodec * D + (ch & 15) * 8);
        }
#pragma unroll
        for (int it = 0; it < 2; ++it) {
            int ch = it * NT1 + tid;
            *(u32x4*)&sW[(ch >> 4) * WST + (ch & 15) * 8] = wv[it];
            *(u32x4*)&sH[(ch >> 4) * WST + (ch & 15) * 8] = hv[it];
        }
        __syncthreads();
        STAMP(0, 1);
        const int np = wave & 3, mg = wave >> 2;
        bf16x8 hb[2][4];
#pragma unroll
        for (int m = 0; m < 2; ++m)
#pragma unroll
            for (int ks = 0; ks < 4; ++ks) hb[m][ks] = *(const bf16x8*)&sH[(mg * 32 + m * 16 + lg) * WST + ks * 32 + grp * 8];
        f32x4 acc[2][2];
#pragma unroll
        for (int m = 0; m < 2; ++m) { acc[m][0] = (f32x4){0.f, 0.f, 0.f, 0.f}; acc[m][1] = (f32x4){0.f, 0.f, 0.f, 0.f}; }
#pragma unroll
        for (int q = 0; q < 2; ++q) {
            const unsigned short* wp = &sW[(grp * 8 + (lg >> 2)) * WST + (lg & 3) * 32 + (np * 2 + q) * 4];
#pragma unroll
            for (int ks = 0; ks < 4; ++ks) {
                typedef __attribute__((ext_vector_type(4))) short s16x4;
                typedef __attribute__((address_space(3))) s16x4* lds_s16x4;
                s16x4 lo = __builtin_amdgcn_ds_read_tr16_b64_v4i16((lds_s16x4)(wp + ks * 32 * WST));
                s16x4 hi = __builtin_amdgcn_ds_read_tr16_b64_v4i16((lds_s16x4)(wp + (ks * 32 + 4) * WST));
                bf16x8 wf = {lo[0], lo[1], lo[2], lo[3], hi[0], hi[1], hi[2], hi[3]};
                acc[0][q] = __builtin_amdgcn_mfma_f32_16x16x32_bf16(wf, hb[0][ks], acc[0][q], 0, 0, 0);
                acc[1][q] = __builtin_amdgcn_mfma_f32_16x16x32_bf16(wf, hb[1][ks], acc[1][q], 0, 0, 0);
            }
        }
#pragma unroll
        for (int m = 0; m < 2; ++m) {
            int node = nb0 + mg * 32 + m * 16 + lg;
            if (node < N) {
                u32x4 v;
                v.x = pack2(acc[m][0][0] * KSC, acc[m][0][1] * KSC); v.y = pack2(acc[m][0][2] * KSC, acc[m][0][3] * KSC);
                v.z = pack2(acc[m][1][0] * KSC, acc[m][1][1] * KSC); v.w = pack2(acc[m][1][2] * KSC, acc[m][1][3] * KSC);
                *(u32x4*)(Bt + (size_t)node * D + grp * 32 + np * 8) = v;
            }
        }
        STAMP(0, 2);
        STAMP_FLUSH(0);
        return;
    }
    {
        int t = (bid - NC - NG) * NT1 + tid;
        if (t < 96 * 64) {
            int f = t >> 6;
            const unsigned short* W;
            int nt, ks;
            if (f < 32) { W = w_e; nt = f >> 2; ks = f & 3; }
            else { int g = f - 32; W = w_h; nt = g >> 3; ks = g & 7; }
            int c = perm_ch(nt, lg);
            int k0 = ks * 32 + 8 * grp;
            unsigned v[8];
#pragma unroll
            for (int j = 0; j < 8; ++j) v[j] = W[(size_t)(k0 + j) * D + c];
            u32x4 o;
            o.x = v[0] | (v[1] << 16); o.y = v[2] | (v[3] << 16); o.z = v[4] | (v[5] << 16); o.w = v[6] | (v[7] << 16);
            frags[t] = o;
        } else if (t < 104 * 64) {
            int f = (t >> 6) - 96;
            const unsigned short* wp = w_e + (size_t)256 * D + f * 16 + lg;
            unsigned v[5];
#pragma unroll
            for (int k = 0; k < 5; ++k) {
                float w = bf1(wp[k * D]) * KSC;
                float hi = rbf(w);
                v[k] = grp == 0 ? f2bf(hi) : (grp == 1 ? f2bf(w - hi) : 0u);
            }
            u32x4 o;
            o.x = v[0] | (v[1] << 16); o.y = v[2] | (v[3] << 16); o.z = v[4]; o.w = 0u;
            frags[t] = o;
        } else {
            for (int n = t - 104 * 64; n < N; n += (gridDim.x - NC - NG) * NT1 - 104 * 64) {
                u32x2 o;
                o.x = (unsigned)pos[n * 3] | ((unsigned)pos[n * 3 + 1] << 16);
                o.y = (unsigned)pos[n * 3 + 2];
                pos4[n] = o;
            }
        }
    }
}

static constexpr int MST = D + 8;
static constexpr int PST = D + 16;
static constexpr int NU_MAX = POOL / 32;
static constexpr int NPR = NU_MAX + T;

__global__ __launch_bounds__(NT2) void k2_kernel(const unsigned short* __restrict__ h,
                                                 const unsigned short* __restrict__ pos,
                                                 const u32x2* __restrict__ e_attr,
                                                 const int* __restrict__ src, const int* __restrict__ dst,
                                                 const unsigned short* __restrict__ w_e,
                                                 const unsigned short* __restrict__ w_x,
                                                 const u32x4* __restrict__ recs, const unsigned* __restrict__ offs,
                                                 const u32x4* __restrict__ frags, const unsigned short* __restrict__ Bt,
                                                 const u32x2* __restrict__ pos4,
                                                 unsigned short* __restrict__ out_h, unsigned short* __restrict__ out_pos,
                                                 int N, int E, int NC, int NB) {
    __shared__ __attribute__((aligned(16))) u32x4 sWh[64 * 64];
    __shared__ __attribute__((aligned(16))) float sProw[NPR * PST];
    __shared__ __attribute__((aligned(16))) float sPpos[NPR * 8];
    __shared__ __attribute__((aligned(16))) unsigned short sH[MT * 16 * MST];
    __shared__ __attribute__((aligned(16))) unsigned short sA[MT * 16 * MST - 8 * MST];
    __shared__ __attribute__((aligned(16))) u32x4 sRec[POOL];
    __shared__ __attribute__((aligned(16))) unsigned short sCf[3][POOL];
    __shared__ __attribute__((aligned(16))) unsigned short sDl[POOL + 32];
    __shared__ float sWx[D];
    __shared__ float sPosT[T * 3 + 8];
    __shared__ int sCnt[64], sOff[64];
    __shared__ int sUf[64], sUl[64];
    __shared__ int sMisc[4];
    unsigned* sKey = (unsigned*)sProw;

    const int tid = threadIdx.x;
    const int wave = __builtin_amdgcn_readfirstlane(tid >> 6);
    const int l = tid & 63;
    const int lg = l & 15, grp = l >> 4;
    const int b = blockIdx.x;
    const int n0 = b * T;
    STAMP_DECL
    STAMP(1, 0);

    constexpr int MAXR = 4;
    const int c8 = tid >> 3, r8 = tid & 7;
    const unsigned ocell = offs[(size_t)(c8 < NC ? c8 : NC - 1) * NB + b];
    u32x4 w1f[8];
    if (wave >= 12) {
#pragma unroll
        for (int i = 0; i < 8; ++i) w1f[i] = frags[((wave & 3) * 8 + i) * 64 + l];
    }
    u32x4 hv = {0u, 0u, 0u, 0u};
    if (wave < MT * 4) {
        int node = n0 + (tid >> 4);
        int nodec = node < N ? node : N - 1;
        hv = *(const u32x4*)(h + (size_t)nodec * D + (tid & 15) * 8);
    }
    unsigned posu, wxu;
    {
        int i = n0 * 3 + (tid < T * 3 ? tid : 0);
        posu = pos[i < N * 3 ? i : N * 3 - 1];
        wxu = w_x[tid & (D - 1)];
    }
    u32x4 wf[4];
#pragma unroll
    for (int i = 0; i < 4; ++i) wf[i] = frags[(96 + (wave & 1) * 4 + i) * 64 + l];
    if (tid < 64) sCnt[tid] = 0;
    if (tid < 4) sMisc[tid] = 0;
    lds_barrier();
    STAMP(1, 6);

    int rank[MAXR];
    unsigned key[MAXR];
    u32x4 ra[MAXR];
    u32x2 ps[MAXR];
    {
        const __amdgpu_buffer_rsrc_t rs = __builtin_amdgcn_make_buffer_rsrc((void*)recs, 0, E * 16, 0x00020000);
        const int cnt = c8 < NC ? (int)(ocell >> 16) : 0;
        const int rbase = c8 * CH + (int)(ocell & 0xffffu);
#pragma unroll
        for (int it = 0; it < MAXR; ++it) {
            int r = r8 + 8 * it;
            ra[it] = __builtin_amdgcn_raw_buffer_load_b128(rs, r < cnt ? (rbase + r) * 16 : 0x7ffffff0, 0, 0);
        }
        if (cnt > 8 * MAXR) sMisc[0] = 1;
#pragma unroll
        for (int it = 0; it < MAXR; ++it) {
            bool valid = r8 + 8 * it < cnt;
            ps[it] = pos4[ra[it].y];
            int dl = ra[it].x >> 16;
            key[it] = ((unsigned)dl << 18) | ((unsigned)(c8 * CH) + (ra[it].x & 0xffffu));
            rank[it] = -1;
            if (valid) rank[it] = atomicAdd(&sCnt[dl], 1) | (dl << 16);
        }
    }
    if (NC > NT2 / 8 && tid == 0) sMisc[0] = 1;
    STAMP(1, 7);
    WSTAMP(8);
    if (wave < MT * 4) *(u32x4*)&sH[(tid >> 4) * MST + (tid & 15) * 8] = hv;
    if (tid < T * 3) sPosT[tid] = bf1((unsigned short)posu);
    if (tid < D) sWx[tid] = bf1((unsigned short)wxu) * KIN;
    unsigned sel[2][4];
#pragma unroll
    for (int hf2 = 0; hf2 < 2; ++hf2)
#pragma unroll
        for (int w = 0; w < 4; ++w) {
            int k0 = grp * 8 + w * 2, tk = hf2 * 16 + lg;
            sel[hf2][w] = (k0 == tk ? 0x3f80u : 0u) | (k0 + 1 == tk ? 0x3f800000u : 0u);
        }
    lds_barrier();
    STAMP(1, 5);
    WSTAMP(9);

    int round_pos = 0;
    float* macc = (float*)sWh;
    u32x4* sRaw = (u32x4*)(sKey + POOL + 64);
    u32x2* sRawP = (u32x2*)(sKey + POOL + 64 + POOL * 4);
    int total;
    bool slow;
    {
        int cntl = sCnt[l];
        int incl = wave_incl_scan(cntl);
        int excl = incl - cntl;
        total = __builtin_amdgcn_readlane(incl, 63);
        slow = sMisc[0] != 0 || total > POOL;
        if (!slow) {
#pragma unroll
            for (int it = 0; it < MAXR; ++it) {
                int dl = rank[it] >> 16;
                int o = __builtin_amdgcn_ds_bpermute((dl & 63) << 2, excl);
                if (rank[it] >= 0) {
                    int p = o + (rank[it] & 0xffff);
                    sKey[p] = key[it]; sRaw[p] = ra[it]; sRawP[p] = ps[it];
                }
            }
            if (wave == 0) sOff[l] = excl;
            if (wave == 1 && l < 4) sKey[total + l] = 0xffffffffu;
        }
    }
    STAMP(1, 9);
    auto rank_step = [&](int tid, int total) {
        if (tid < total) {
            u32x4 rr = sRaw[tid];
            u32x2 pp = sRawP[tid];
            unsigned ky = sKey[tid];
            int dl = rr.x >> 16;
            int o = sOff[dl], end = o + sCnt[dl];
            int j0 = o & ~3;
            int p = j0;
            for (int j = j0; j < end; j += 4) {
                u32x4 k4 = *(const u32x4*)&sKey[j];
                p += (k4.x < ky ? 1 : 0) + (k4.y < ky ? 1 : 0) + (k4.z < ky ? 1 : 0) + (k4.w < ky ? 1 : 0);
            }
            float d0 = rbf(sPosT[dl * 3 + 0] - bf_lo(pp.x));
            float d1 = rbf(sPosT[dl * 3 + 1] - bf_hi(pp.x));
            float d2 = rbf(sPosT[dl * 3 + 2] - bf_lo(pp.y));
            float q = rbf(rbf(d0 * d0) + rbf(d1 * d1) + rbf(d2 * d2));
            u32x4 rec;
            rec.x = (__float_as_uint(q) >> 16) | (rr.z << 16);
            rec.y = (rr.z >> 16) | (rr.w << 16);
            rec.z = (rr.w >> 16) | ((unsigned)dl << 16);
            rec.w = rr.y;
            sRec[p] = rec;
            sCf[0][p] = (unsigned short)(__float_as_uint(d0) >> 16);
            sCf[1][p] = (unsigned short)(__float_as_uint(d1) >> 16);
            sCf[2][p] = (unsigned short)(__float_as_uint(d2) >> 16);
            sDl[p] = (unsigned short)dl;
        }
        if (tid < 32) sDl[total + tid] = 0xffffu;
        if (tid >= 64 && tid < 64 + T) {
            int n = tid - 64;
            int o = sOff[n], cnt = sCnt[n];
            sUf[n] = o >> 5;
            sUl[n] = cnt > 0 ? (o + cnt - 1) >> 5 : -1;
        }
    };
    lds_barrier();
    STAMP(1, 10);
    WSTAMP(10);
    if (!slow) rank_step(tid, total);
    WSTAMP(11);
    if (wave >= 12) {
        const int np = wave & 3;
#pragma unroll
        for (int mt = 0; mt < MT; ++mt) {
            f32x4 a0 = {0.f, 0.f, 0.f, 0.f}, a1 = {0.f, 0.f, 0.f, 0.f};
#pragma unroll
            for (int ks = 0; ks < 4; ++ks) {
                bf16x8 hb = *(const bf16x8*)&sH[(mt * 16 + lg) * MST + ks * 32 + grp * 8];
                a0 = __builtin_amdgcn_mfma_f32_16x16x32_bf16(__builtin_bit_cast(bf16x8, w1f[ks]), hb, a0, 0, 0, 0);
                a1 = __builtin_amdgcn_mfma_f32_16x16x32_bf16(__builtin_bit_cast(bf16x8, w1f[4 + ks]), hb, a1, 0, 0, 0);
            }
            int row = mt * 16 + lg;
            if (row < T) {
                u32x4 v;
                v.x = pack2(a0[0] * KSC, a0[1] * KSC); v.y = pack2(a0[2] * KSC, a0[3] * KSC);
                v.z = pack2(a1[0] * KSC, a1[1] * KSC); v.w = pack2(a1[2] * KSC, a1[3] * KSC);
                *(u32x4*)&sA[row * MST + grp * 32 + np * 8] = v;
            }
        }
    }
    if (slow) {
        for (int i = tid; i < T * PST; i += NT2) macc[i] = 0.f;
    } else {
#pragma unroll
        for (int i = 0; i < 4; ++i) {
            unsigned lds_off = __builtin_amdgcn_readfirstlane((unsigned)(uintptr_t)(sWh + i * NT2 + wave * 64));
            const u32x4* gp = frags + 32 * 64 + i * NT2 + tid;
            asm volatile("s_mov_b32 m0, %0\n\ts_nop 0\n\tglobal_load_lds_dwordx4 %1, off" :: "s"(lds_off), "v"(gp) : "memory", "m0");
        }
    }
    STAMP(1, 11);
    WSTAMP(12);
    for (;;) {
        int tid = threadIdx.x;
        asm volatile("" : "+v"(tid));
        const int l = tid & 63;
        const int lg = l & 15, grp = l >> 4;
        if (slow) {
            __syncthreads();
            if (tid < 64) sCnt[tid] = 0;
            if (tid == 0) sMisc[1] = 0;
            __syncthreads();
            int e = round_pos + tid;
            round_pos += NT2;
            int rk = -1, dl = 0;
            u32x4 rr = {0u, 0u, 0u, 0u};
            u32x2 pp = {0u, 0u};
            if (e < E) {
                dl = dst[e] - n0;
                if (dl >= 0 && dl < T) {
                    int s = src[e];
                    u32x2 ea = e_attr[e];
                    rr.x = (unsigned)dl << 16; rr.y = (unsigned)s; rr.z = ea.x; rr.w = ea.y;
                    pp = pos4[s];
                    rk = atomicAdd(&sCnt[dl], 1);
                }
            }
            __syncthreads();
            int cntl = sCnt[l];
            int incl = wave_incl_scan(cntl);
            int excl = incl - cntl;
            total = __builtin_amdgcn_readlane(incl, 63);
            int o = __builtin_amdgcn_ds_bpermute((dl & 63) << 2, excl);
            if (rk >= 0) { int p = o + rk; sKey[p] = ((unsigned)dl << 18) | (unsigned)tid; sRaw[p] = rr; sRawP[p] = pp; }
            if (wave == 0) sOff[l] = excl;
            if (wave == 1 && l < 4) sKey[total + l] = 0xffffffffu;
            __syncthreads();
            rank_step(tid, total);
        }
        lds_barrier();
        STAMP(1, 1);

        const int NW = ((total + 31) >> 5) * 2;
        int wsi = 0;
        WSTAMP(wsi); wsi++;
        auto fetch = [&](int g, u32x4 (&R)[2], u32x4 (&X)[2][2]) {
            int gc = g < NW ? g : NW - 1;
            int e = (gc >> 1) * 32 + lg;
            R[0] = sRec[e < total ? e : total - 1];
            R[1] = sRec[e + 16 < total ? e + 16 : total - 1];
#pragma unroll
            for (int bt = 0; bt < 2; ++bt) {
                const unsigned short* brow = Bt + (size_t)R[bt].w * D + (gc & 1) * 64 + grp * 8;
                X[bt][0] = *(const u32x4*)brow; X[bt][1] = *(const u32x4*)(brow + 32);
            }
        };
        auto body = [&](int g, const u32x4 (&R)[2], const u32x4 (&X)[2][2], u32x4 (&Rn)[2], u32x4 (&Xn)[2][2]) {
            const int u = g >> 1, hg = g & 1;
            const int p0 = u * 32;
            unsigned S[2][4][2];
            fetch(g + 16, Rn, Xn);
            __builtin_amdgcn_sched_barrier(0);
#pragma unroll
            for (int bt = 0; bt < 2; ++bt) {
                const u32x4 rec = R[bt];
                const unsigned short* arow = &sA[(rec.z >> 16) * MST + hg * 64 + grp * 8];
                const u32x4 F = {rec.x, rec.y, rec.z & 0xffffu, 0u};
                f32x4 acc[4];
#pragma unroll
                for (int g4 = 0; g4 < 4; ++g4) {
                    u32x4 Y = *(const u32x4*)(arow + (g4 >> 1) * 32);
                    u32x4 Sl = {sel[g4 & 1][0], sel[g4 & 1][1], sel[g4 & 1][2], sel[g4 & 1][3]};
                    f32x4 a = {0.f, 0.f, 0.f, 0.f};
                    a = __builtin_amdgcn_mfma_f32_16x16x32_bf16(__builtin_bit_cast(bf16x8, F), __builtin_bit_cast(bf16x8, wf[g4]), a, 0, 0, 0);
                    a = __builtin_amdgcn_mfma_f32_16x16x32_bf16(__builtin_bit_cast(bf16x8, Y), __builtin_bit_cast(bf16x8, Sl), a, 0, 0, 0);
                    a = __builtin_amdgcn_mfma_f32_16x16x32_bf16(__builtin_bit_cast(bf16x8, X[bt][g4 >> 1]), __builtin_bit_cast(bf16x8, Sl), a, 0, 0, 0);
                    acc[g4] = a;
                }
#pragma unroll
                for (int g4 = 0; g4 < 4; ++g4) {
#pragma unroll
                    for (int hh = 0; hh < 2; ++hh) {
                        f32x2 x = {acc[g4][hh * 2], acc[g4][hh * 2 + 1]};
                        f32x2 ex = {__builtin_amdgcn_exp2f(x[0]), __builtin_amdgcn_exp2f(x[1])};
                        ex += 1.0f;
                        f32x2 rc = {__builtin_amdgcn_rcpf(ex[0]), __builtin_amdgcn_rcpf(ex[1])};
                        S[bt][g4][hh] = cvt_pk(x * rc);
                    }
                }
                __builtin_amdgcn_sched_barrier(0);
            }
            WSTAMP(wsi); wsi++;
            {
                int l = threadIdx.x;
                asm volatile("" : "+v"(l));
                l &= 63;
                const int lg = l & 15, grp = l >> 4;
                int lastp = p0 + 31 < total ? p0 + 31 : total - 1;
                int nlo = __builtin_amdgcn_readfirstlane((int)sDl[p0]);
                int nhi = __builtin_amdgcn_readfirstlane((int)sDl[lastp]);
                u32x2 dv0 = *(const u32x2*)&sDl[p0 + 4 * grp];
                u32x2 dv1 = *(const u32x2*)&sDl[p0 + 16 + 4 * grp];
                int c = lg & 3;
                int ci = c > 0 ? c - 1 : 0;
                u32x2 cf0 = *(const u32x2*)&sCf[ci][p0 + 4 * grp];
                u32x2 cf1 = *(const u32x2*)&sCf[ci][p0 + 16 + 4 * grp];
                if (c == 0) { cf0 = (u32x2){0x3f803f80u, 0x3f803f80u}; cf1 = cf0; }
                float wxv[4];
#pragma unroll
                for (int j = 0; j < 4; ++j) wxv[j] = sWx[(hg * 4 + j) * 16 + lg];
                for (int base = nlo; base <= nhi; base += 4) {
                    unsigned tg = (unsigned)(base + (lg >> 2)) * 0x10001u;
                    u32x4 G;
                    G.x = eq_sel16(dv0.x, tg, cf0.x); G.y = eq_sel16(dv0.y, tg, cf0.y);
                    G.z = eq_sel16(dv1.x, tg, cf1.x); G.w = eq_sel16(dv1.y, tg, cf1.y);
                    int node = base + grp;
                    bool act = node <= nhi;
                    float* prow = &sProw[(u + node) * PST + hg * 64 + lg];
                    f32x4 M[4];
#pragma unroll
                    for (int j = 0; j < 4; ++j) {
                        u32x4 Sv = {S[0][j][0], S[0][j][1], S[1][j][0], S[1][j][1]};
                        f32x4 z = {0.f, 0.f, 0.f, 0.f};
                        M[j] = __builtin_amdgcn_mfma_f32_16x16x32_bf16(__builtin_bit_cast(bf16x8, G), __builtin_bit_cast(bf16x8, Sv), z, 0, 0, 0);
                    }
                    float q1 = 0.f, q2 = 0.f, q3 = 0.f;
#pragma unroll
                    for (int j = 0; j < 4; ++j) {
                        q1 = fmaf(M[j][1], wxv[j], q1); q2 = fmaf(M[j][2], wxv[j], q2); q3 = fmaf(M[j][3], wxv[j], q3);
                    }
                    q1 = row16_sum(q1); q2 = row16_sum(q2); q3 = row16_sum(q3);
                    if (act) {
#pragma unroll
                        for (int j = 0; j < 4; ++j) prow[j * 16] = M[j][0];
                        if (lg < 3) sPpos[(u + node) * 8 + hg * 4 + lg] = lg == 0 ? q1 : (lg == 1 ? q2 : q3);
                    }
                }
            }
            WSTAMP(wsi); wsi++;
        };
        if (NW > 0) {
            u32x4 RA[2], XA[2][2], RB[2], XB[2][2];
            int g = wave;
            fetch(g, RA, XA);
            __builtin_amdgcn_s_setprio(3);
            for (;;) {
                if (g >= NW) break;
                body(g, RA, XA, RB, XB); g += 16;
                if (g < 32) __builtin_amdgcn_s_setprio(2); else __builtin_amdgcn_s_setprio(0);
                if (g >= NW) break;
                body(g, RB, XB, RA, XA); g += 16;
                if (g < 48) __builtin_amdgcn_s_setprio(1); else __builtin_amdgcn_s_setprio(0);
            }
            __builtin_amdgcn_s_setprio(0);
        }
        STAMP(1, 2);
        asm volatile("s_waitcnt vmcnt(0)" ::: "memory");
        __syncthreads();
        STAMP(1, 3);
        if (!slow) break;
        tid = threadIdx.x;
        asm volatile("" : "+v"(tid));
        for (int i = tid; i < T * 32; i += NT2) {
            int n = i >> 5, c4 = i & 31;
            f32x4 a = *(const f32x4*)&macc[n * PST + c4 * 4];
            for (int uu = sUf[n]; uu <= sUl[n]; ++uu) a += *(const f32x4*)&sProw[(uu + n) * PST + c4 * 4];
            *(f32x4*)&macc[n * PST + c4 * 4] = a;
        }
        if (tid < T * 3) {
            int n = tid / 3, c = tid - n * 3;
            float a = macc[n * PST + D + c];
            for (int uu = sUf[n]; uu <= sUl[n]; ++uu) a += sPpos[(uu + n) * 8 + c] + sPpos[(uu + n) * 8 + 4 + c];
            macc[n * PST + D + c] = a;
        }
        if (round_pos >= E) break;
    }
    if (slow) {
        int tid = threadIdx.x;
        asm volatile("" : "+v"(tid));
        __syncthreads();
        for (int i = tid; i < T * 32; i += NT2) {
            int n = i >> 5, c4 = i & 31;
            *(f32x4*)&sProw[n * PST + c4 * 4] = *(const f32x4*)&macc[n * PST + c4 * 4];
        }
        if (tid < T * 3) { int n = tid / 3, c = tid - n * 3; sPpos[n * 8 + c] = macc[n * PST + D + c]; sPpos[n * 8 + 4 + c] = 0.f; }
        if (tid < T) { sUf[tid] = 0; sUl[tid] = 0; }
        __syncthreads();
#pragma unroll
        for (int i = 0; i < 4; ++i) sWh[i * NT2 + tid] = frags[32 * 64 + i * NT2 + tid];
        __syncthreads();
    }

    if (wave < MT * 4) {
        int l = threadIdx.x;
        asm volatile("" : "+v"(l));
        l &= 63;
        const int lg = l & 15, grp = l >> 4;
        int mt = wave >> 2, np = wave & 3;
        int row = mt * 16 + lg;
        int node = n0 + row;
        bool valid = (row < T) && (node < N);
        int uf = valid ? sUf[row] : 0, ul = valid ? sUl[row] : -1;
        f32x4 a0 = {0.f, 0.f, 0.f, 0.f}, a1 = {0.f, 0.f, 0.f, 0.f};
#pragma unroll
        for (int ks = 0; ks < 8; ++ks) {
            bf16x8 xb;
            if (ks < 4) xb = *(const bf16x8*)&sH[row * MST + ks * 32 + grp * 8];
            else {
                f32x4 m0 = {0.f, 0.f, 0.f, 0.f}, m1 = {0.f, 0.f, 0.f, 0.f};
                for (int uu = uf; uu <= ul; ++uu) {
                    const float* pr = &sProw[(uu + row) * PST + (ks - 4) * 32 + grp * 8];
                    m0 += *(const f32x4*)pr; m1 += *(const f32x4*)(pr + 4);
                }
                u32x4 v;
                m0 *= KIN; m1 *= KIN;
                v.x = cvt_pk((f32x2){m0[0], m0[1]}); v.y = cvt_pk((f32x2){m0[2], m0[3]});
                v.z = cvt_pk((f32x2){m1[0], m1[1]}); v.w = cvt_pk((f32x2){m1[2], m1[3]});
                xb = __builtin_bit_cast(bf16x8, v);
            }
            a0 = __builtin_amdgcn_mfma_f32_16x16x32_bf16(__builtin_bit_cast(bf16x8, sWh[((np * 2) * 8 + ks) * 64 + l]), xb, a0, 0, 0, 0);
            a1 = __builtin_amdgcn_mfma_f32_16x16x32_bf16(__builtin_bit_cast(bf16x8, sWh[((np * 2 + 1) * 8 + ks) * 64 + l]), xb, a1, 0, 0, 0);
        }
        if (valid) {
            u32x4 hv2 = *(const u32x4*)&sH[row * MST + grp * 32 + np * 8];
            u32x4 v;
            v.x = pack2(bf_lo(hv2.x) + silu_f(a0[0]), bf_hi(hv2.x) + silu_f(a0[1]));
            v.y = pack2(bf_lo(hv2.y) + silu_f(a0[2]), bf_hi(hv2.y) + silu_f(a0[3]));
            v.z = pack2(bf_lo(hv2.z) + silu_f(a1[0]), bf_hi(hv2.z) + silu_f(a1[1]));
            v.w = pack2(bf_lo(hv2.w) + silu_f(a1[2]), bf_hi(hv2.w) + silu_f(a1[3]));
            *(u32x4*)(out_h + (size_t)node * D + grp * 32 + np * 8) = v;
        }
    } else {
        int t = threadIdx.x;
        asm volatile("" : "+v"(t));
        t -= MT * 4 * 64;
        if (t < T * 3 && n0 * 3 + t < N * 3) {
            int n = t / 3, c = t - n * 3;
            float a = 0.f;
            for (int uu = sUf[n]; uu <= sUl[n]; ++uu) a += sPpos[(uu + n) * 8 + c] + sPpos[(uu + n) * 8 + 4 + c];
            out_pos[(size_t)n0 * 3 + t] = (unsigned short)f2bf(sPosT[t] + a);
        }
    }
    STAMP(1, 4);
    STAMP_FLUSH(1);
    WSTAMP_FLUSH();
}

extern "C" void kernel_launch(void* const* d_in, const int* in_sizes, int n_in,
                              void* d_out, int out_size, void* d_ws, size_t ws_size,
                              hipStream_t stream) {
    const unsigned short* h = (const unsigned short*)d_in[0];
    const unsigned short* pos = (const unsigned short*)d_in[1];
    const u32x2* e_attr = (const u32x2*)d_in[2];
    const int* src = (const int*)d_in[3];
    const int* dst = (const int*)d_in[4];
    const unsigned short* w_e = (const unsigned short*)d_in[5];
    const unsigned short* w_x = (const unsigned short*)d_in[6];
    const unsigned short* w_h = (const unsigned short*)d_in[7];
    const int N = in_sizes[1] / 3;
    const int E = in_sizes[3];
    const int NB = (N + T - 1) / T;
    const int NC = (E + CH - 1) / CH;
    const int NG = (N + 127) / 128;

    char* ws = (char*)d_ws;
    size_t off = 0;
    auto take = [&](size_t bytes) { char* p = ws + off; off += (bytes + 255) / 256 * 256; return p; };
    u32x4* frags = (u32x4*)take((size_t)104 * 64 * 16);
    unsigned short* Bt = (unsigned short*)take((size_t)N * D * 2);
    u32x2* pos4 = (u32x2*)take((size_t)N * 8);
    u32x4* recs = (u32x4*)take((size_t)NC * CH * 16);
    unsigned* offs = (unsigned*)take((size_t)NC * NB * 4);

    unsigned short* out_h = (unsigned short*)d_out;
    unsigned short* out_pos = out_h + (size_t)N * D;

    size_t lds1 = (size_t)CH * 16 + (size_t)NB * 8;
    if (lds1 < (size_t)2 * 128 * (D + 8) * 2) lds1 = (size_t)2 * 128 * (D + 8) * 2;
    (void)hipFuncSetAttribute((const void*)k1_kernel, hipFuncAttributeMaxDynamicSharedMemorySize, (int)lds1);
    k1_kernel<<<NC + NG + 16, NT1, lds1, stream>>>(h, pos, e_attr, src, dst, w_e, w_h, recs, offs, frags, Bt, pos4,
                                                    N, E, NB, NC, NG);
    k2_kernel<<<NB, NT2, 0, stream>>>(h, pos, e_attr, src, dst, w_e, w_x, recs, offs, frags, Bt, pos4,
                                      out_h, out_pos, N, E, NC, NB);
}
```

```cpp
#include <hip/hip_runtime.h>
#include <hip/hip_bf16.h>
#include <stdint.h>

typedef __attribute__((ext_vector_type(8))) short bf16x8;
typedef __attribute__((ext_vector_type(4))) float f32x4;
typedef __attribute__((ext_vector_type(2))) float f32x2;
typedef __attribute__((ext_vector_type(4))) unsigned u32x4;
typedef __attribute__((ext_vector_type(2))) unsigned u32x2;
typedef __attribute__((ext_vector_type(2))) __bf16 bf16x2v;
typedef __attribute__((ext_vector_type(2))) unsigned short u16x2;

#define STAMP_DECL
#define STAMP(K, i)
#define STAMP_FLUSH(K)
#define WSTAMP(i)
#define WSTAMP_FLUSH()

static constexpr int D = 128;
static constexpr int T = 40;
static constexpr int MT = 3;
static constexpr int POOL = 1024;
static constexpr int CH = 2048;
static constexpr int NT1 = 1024;
static constexpr int NT2 = 1024;

static constexpr float KSC = -1.44269504088896340736f;
static constexpr float KIN = -0.69314718055994530942f;

__device__ __forceinline__ float bf_lo(unsigned u) { return __uint_as_float(u << 16); }
__device__ __forceinline__ float bf_hi(unsigned u) { return __uint_as_float(u & 0xffff0000u); }
__device__ __forceinline__ float bf1(unsigned short u) { return __uint_as_float(((unsigned)u) << 16); }
__device__ __forceinline__ unsigned f2bf(float f) {
    __hip_bfloat16 b = __float2bfloat16(f);
    return (unsigned)*reinterpret_cast<unsigned short*>(&b);
}
__device__ __forceinline__ float rbf(float f) { return __uint_as_float(f2bf(f) << 16); }
__device__ __forceinline__ unsigned pack2(float lo, float hi) { return f2bf(lo) | (f2bf(hi) << 16); }

__device__ __forceinline__ unsigned cvt_pk(f32x2 v) {
    return __builtin_bit_cast(unsigned, __builtin_convertvector(v, bf16x2v));
}
__device__ __forceinline__ unsigned eq_sel16(unsigned a, unsigned b, unsigned c) {
    unsigned d = a ^ b, m;
    asm("v_pk_min_u16 %0, %1, %2" : "=v"(m) : "v"(d), "v"(0x00010001u));
    asm("v_pk_sub_u16 %0, %1, %2" : "=v"(m) : "v"(m), "v"(0x00010001u));
    return m & c;
}
__device__ __forceinline__ void lds_barrier() {
    asm volatile("s_waitcnt lgkmcnt(0)" ::: "memory");
    __builtin_amdgcn_s_barrier();
    asm volatile("" ::: "memory");
}
__device__ __forceinline__ float silu_f(float x) {
    float e = __builtin_amdgcn_exp2f(x * -1.44269504088896340736f);
    return x * __builtin_amdgcn_rcpf(1.0f + e);
}

template <int CTRL> __device__ __forceinline__ float dpp_mov(float v) {
    return __builtin_bit_cast(float, __builtin_amdgcn_update_dpp(0, __builtin_bit_cast(int, v), CTRL, 0xf, 0xf, false));
}
__device__ __forceinline__ float row16_sum(float v) {
    v += dpp_mov<0x128>(v);
    v += dpp_mov<0x124>(v);
    v += dpp_mov<0x122>(v);
    v += dpp_mov<0x121>(v);
    return v;
}
__device__ __forceinline__ int wave_incl_scan(int x) {
    x += __builtin_amdgcn_update_dpp(0, x, 0x111, 0xf, 0xf, false);
    x += __builtin_amdgcn_update_dpp(0, x, 0x112, 0xf, 0xf, false);
    x += __builtin_amdgcn_update_dpp(0, x, 0x114, 0xf, 0xf, false);
    x += __builtin_amdgcn_update_dpp(0, x, 0x118, 0xf, 0xf, false);
    x += __builtin_amdgcn_update_dpp(0, x, 0x142, 0xa, 0xf, false);
    x += __builtin_amdgcn_update_dpp(0, x, 0x143, 0xc, 0xf, false);
    return x;
}

__device__ __forceinline__ int perm_ch(int nt, int i) { return (i >> 2) * 32 + nt * 4 + (i & 3); }

__global__ __launch_bounds__(NT1) void k1_kernel(const unsigned short* __restrict__ h,
                                                 const unsigned short* __restrict__ pos,
                                                 const u32x2* __restrict__ e_attr,
                                                 const int* __restrict__ src, const int* __restrict__ dst,
                                                 const unsigned short* __restrict__ w_e,
                                                 const unsigned short* __restrict__ w_h,
                                                 u32x4* __restrict__ recs, unsigned* __restrict__ offs,
                                                 u32x4* __restrict__ frags,
                                                 unsigned short* __restrict__ Bt, u32x2* __restrict__ pos4,
                                                 int N, int E, int NB, int NC, int NG) {
    extern __shared__ __attribute__((aligned(16))) char smem[];
    const int tid = threadIdx.x;
    const int bid = blockIdx.x;
    STAMP_DECL
    STAMP(0, 0);
    if (bid < NC) {
        u32x4* sRA = (u32x4*)smem;
        int* lcnt = (int*)(smem + CH * 16);
        int* loff = lcnt + NB;
        const int ebase = bid * CH;
        int dd[CH / NT1], ss[CH / NT1]; u32x2 ea[CH / NT1];
#pragma unroll
        for (int it = 0; it < CH / NT1; ++it) {
            int e = ebase + it * NT1 + tid;
            int ec = e < E ? e : E - 1;
            dd[it] = dst[ec]; ss[it] = src[ec]; ea[it] = e_attr[ec];
        }
        for (int i = tid; i < NB; i += NT1) lcnt[i] = 0;
        __syncthreads();
        STAMP(0, 3);
        u32x4 ra[CH / NT1]; int bk[CH / NT1], rk[CH / NT1];
#pragma unroll
        for (int it = 0; it < CH / NT1; ++it) {
            int el = it * NT1 + tid;
            int e = ebase + el;
            bk[it] = -1;
            if (e < E) {
                int d = dd[it];
                int b = d / T;
                int dl = d - b * T;
                rk[it] = atomicAdd(&lcnt[b], 1);
                bk[it] = b;
                ra[it].x = ((unsigned)dl << 16) | (unsigned)el; ra[it].y = (unsigned)ss[it]; ra[it].z = ea[it].x; ra[it].w = ea[it].y;
            }
        }
        __syncthreads();
        STAMP(0, 4);
        if (tid < 64) {
            int carry = 0;
            for (int base = 0; base < NB; base += 256) {
                int i = base + tid * 4;
                int v0 = i < NB ? lcnt[i] : 0, v1 = i + 1 < NB ? lcnt[i + 1] : 0;
                int v2 = i + 2 < NB ? lcnt[i + 2] : 0, v3 = i + 3 < NB ? lcnt[i + 3] : 0;
                int sum = v0 + v1 + v2 + v3;
                int x = wave_incl_scan(sum);
                int e = carry + x - sum;
                if (i < NB) loff[i] = e;
                if (i + 1 < NB) loff[i + 1] = e + v0;
                if (i + 2 < NB) loff[i + 2] = e + v0 + v1;
                if (i + 3 < NB) loff[i + 3] = e + v0 + v1 + v2;
                carry += __builtin_amdgcn_readlane(x, 63);
            }
        }
        __syncthreads();
        STAMP(0, 5);
#pragma unroll
        for (int it = 0; it < CH / NT1; ++it)
            if (bk[it] >= 0) sRA[loff[bk[it]] + rk[it]] = ra[it];
        for (int i = tid; i < NB; i += NT1) offs[(size_t)bid * NB + i] = (unsigned)loff[i] | ((unsigned)lcnt[i] << 16);
        __syncthreads();
        STAMP(0, 1);
        const int nvalid = (E - ebase) < CH ? (E - ebase) : CH;
#pragma unroll
        for (int it = 0; it < CH / NT1; ++it) {
            int p = it * NT1 + tid;
            if (p < nvalid) recs[(size_t)ebase + p] = sRA[p];
        }
        STAMP(0, 2);
        STAMP_FLUSH(0);
        return;
    }
    const int wave = tid >> 6, l = tid & 63;
    const int lg = l & 15, grp = l >> 4;
    if (bid < NC + NG) {
        constexpr int WST = D + 8;
        unsigned short* sW = (unsigned short*)smem;
        unsigned short* sH = sW + 128 * WST;
        const int nb0 = (bid - NC) * 128;
        const u32x4* w2 = (const u32x4*)(w_e + (size_t)128 * D);
        u32x4 wv[2], hv[2];
#pragma unroll
        for (int it = 0; it < 2; ++it) {
            int ch = it * NT1 + tid;
            wv[it] = w2[ch];
            int node = nb0 + (ch >> 4);
            int nodec = node < N ? node : N - 1;
            hv[it] = *(const u32x4*)(h + (size_t)nodec * D + (ch & 15) * 8);
        }
#pragma unroll
        for (int it = 0; it < 2; ++it) {
            int ch = it * NT1 + tid;
            *(u32x4*)&sW[(ch >> 4) * WST + (ch & 15) * 8] = wv[it];
            *(u32x4*)&sH[(ch >> 4) * WST + (ch & 15) * 8] = hv[it];
        }
        __syncthreads();
        STAMP(0, 1);
        const int np = wave & 3, mg = wave >> 2;
        bf16x8 hb[2][4];
#pragma unroll
        for (int m = 0; m < 2; ++m)
#pragma unroll
            for (int ks = 0; ks < 4; ++ks) hb[m][ks] = *(const bf16x8*)&sH[(mg * 32 + m * 16 + lg) * WST + ks * 32 + grp * 8];
        f32x4 acc[2][2];
#pragma unroll
        for (int m = 0; m < 2; ++m) { acc[m][0] = (f32x4){0.f, 0.f, 0.f, 0.f}; acc[m][1] = (f32x4){0.f, 0.f, 0.f, 0.f}; }
#pragma unroll
        for (int q = 0; q < 2; ++q) {
            const unsigned short* wp = &sW[(grp * 8 + (lg >> 2)) * WST + (lg & 3) * 32 + (np * 2 + q) * 4];
#pragma unroll
            for (int ks = 0; ks < 4; ++ks) {
                typedef __attribute__((ext_vector_type(4))) short s16x4;
                typedef __attribute__((address_space(3))) s16x4* lds_s16x4;
                s16x4 lo = __builtin_amdgcn_ds_read_tr16_b64_v4i16((lds_s16x4)(wp + ks * 32 * WST));
                s16x4 hi = __builtin_amdgcn_ds_read_tr16_b64_v4i16((lds_s16x4)(wp + (ks * 32 + 4) * WST));
                bf16x8 wf = {lo[0], lo[1], lo[2], lo[3], hi[0], hi[1], hi[2], hi[3]};
                acc[0][q] = __builtin_amdgcn_mfma_f32_16x16x32_bf16(wf, hb[0][ks], acc[0][q], 0, 0, 0);
                acc[1][q] = __builtin_amdgcn_mfma_f32_16x16x32_bf16(wf, hb[1][ks], acc[1][q], 0, 0, 0);
            }
        }
#pragma unroll
        for (int m = 0; m < 2; ++m) {
            int node = nb0 + mg * 32 + m * 16 + lg;
            if (node < N) {
                u32x4 v;
                v.x = pack2(acc[m][0][0] * KSC, acc[m][0][1] * KSC); v.y = pack2(acc[m][0][2] * KSC, acc[m][0][3] * KSC);
                v.z = pack2(acc[m][1][0] * KSC, acc[m][1][1] * KSC); v.w = pack2(acc[m][1][2] * KSC, acc[m][1][3] * KSC);
                *(u32x4*)(Bt + (size_t)node * D + grp * 32 + np * 8) = v;
            }
        }
        STAMP(0, 2);
        STAMP_FLUSH(0);
        return;
    }
    {
        int t = (bid - NC - NG) * NT1 + tid;
        if (t < 96 * 64) {
            int f = t >> 6;
            const unsigned short* W;
            int nt, ks;
            if (f < 32) { W = w_e; nt = f >> 2; ks = f & 3; }
            else { int g = f - 32; W = w_h; nt = g >> 3; ks = g & 7; }
            int c = perm_ch(nt, lg);
            int k0 = ks * 32 + 8 * grp;
            unsigned v[8];
#pragma unroll
            for (int j = 0; j < 8; ++j) v[j] = W[(size_t)(k0 + j) * D + c];
            u32x4 o;
            o.x = v[0] | (v[1] << 16); o.y = v[2] | (v[3] << 16); o.z = v[4] | (v[5] << 16); o.w = v[6] | (v[7] << 16);
            frags[t] = o;
        } else if (t < 104 * 64) {
            int f = (t >> 6) - 96;
            const unsigned short* wp = w_e + (size_t)256 * D + f * 16 + lg;
            unsigned v[5];
#pragma unroll
            for (int k = 0; k < 5; ++k) {
                float w = bf1(wp[k * D]) * KSC;
                float hi = rbf(w);
                v[k] = grp == 0 ? f2bf(hi) : (grp == 1 ? f2bf(w - hi) : 0u);
            }
            u32x4 o;
            o.x = v[0] | (v[1] << 16); o.y = v[2] | (v[3] << 16); o.z = v[4]; o.w = 0u;
            frags[t] = o;
        } else {
            for (int n = t - 104 * 64; n < N; n += (gridDim.x - NC - NG) * NT1 - 104 * 64) {
                u32x2 o;
                o.x = (unsigned)pos[n * 3] | ((unsigned)pos[n * 3 + 1] << 16);
                o.y = (unsigned)pos[n * 3 + 2];
                pos4[n] = o;
            }
        }
    }
}

static constexpr int MST = D + 8;
static constexpr int PST = D + 16;
static constexpr int NU_MAX = POOL / 32;
static constexpr int NPR = NU_MAX + T;

__global__ __launch_bounds__(NT2) void k2_kernel(const unsigned short* __restrict__ h,
                                                 const unsigned short* __restrict__ pos,
                                                 const u32x2* __restrict__ e_attr,
                                                 const int* __restrict__ src, const int* __restrict__ dst,
                                                 const unsigned short* __restrict__ w_e,
                                                 const unsigned short* __restrict__ w_x,
                                                 const u32x4* __restrict__ recs, const unsigned* __restrict__ offs,
                                                 const u32x4* __restrict__ frags, const unsigned short* __restrict__ Bt,
                                                 const u32x2* __restrict__ pos4,
                                                 unsigned short* __restrict__ out_h, unsigned short* __restrict__ out_pos,
                                                 int N, int E, int NC, int NB) {
    __shared__ __attribute__((aligned(16))) u32x4 sWh[64 * 64];
    __shared__ __attribute__((aligned(16))) float sProw[NPR * PST];
    __shared__ __attribute__((aligned(16))) float sPpos[NPR * 8];
    __shared__ __attribute__((aligned(16))) unsigned short sH[MT * 16 * MST];
    __shared__ __attribute__((aligned(16))) unsigned short sA[MT * 16 * MST - 8 * MST];
    __shared__ __attribute__((aligned(16))) u32x4 sRec[POOL];
    __shared__ __attribute__((aligned(16))) unsigned short sCf[3][POOL];
    __shared__ __attribute__((aligned(16))) unsigned short sDl[POOL + 32];
    __shared__ float sWx[D];
    __shared__ float sPosT[T * 3 + 8];
    __shared__ int sCnt[64], sOff[64];
    __shared__ int sUf[64], sUl[64];
    __shared__ int sMisc[4];
    unsigned* sKey = (unsigned*)sProw;

    const int tid = threadIdx.x;
    const int wave = __builtin_amdgcn_readfirstlane(tid >> 6);
    const int l = tid & 63;
    const int lg = l & 15, grp = l >> 4;
    const int b = blockIdx.x;
    const int n0 = b * T;
    STAMP_DECL
    STAMP(1, 0);

    constexpr int MAXR = 4;
    const int c8 = tid >> 3, r8 = tid & 7;
    const unsigned ocell = offs[(size_t)(c8 < NC ? c8 : NC - 1) * NB + b];
    u32x4 w1f[8];
    if (wave >= 12) {
#pragma unroll
        for (int i = 0; i < 8; ++i) w1f[i] = frags[((wave & 3) * 8 + i) * 64 + l];
    }
    u32x4 hv = {0u, 0u, 0u, 0u};
    if (wave < MT * 4) {
        int node = n0 + (tid >> 4);
        int nodec = node < N ? node : N - 1;
        hv = *(const u32x4*)(h + (size_t)nodec * D + (tid & 15) * 8);
    }
    unsigned posu, wxu;
    {
        int i = n0 * 3 + (tid < T * 3 ? tid : 0);
        posu = pos[i < N * 3 ? i : N * 3 - 1];
        wxu = w_x[tid & (D - 1)];
    }
    u32x4 wf[4];
#pragma unroll
    for (int i = 0; i < 4; ++i) wf[i] = frags[(96 + (wave & 1) * 4 + i) * 64 + l];
    if (tid < 64) sCnt[tid] = 0;
    if (tid < 4) sMisc[tid] = 0;
    lds_barrier();
    STAMP(1, 6);

    int rank[MAXR];
    unsigned key[MAXR];
    u32x4 ra[MAXR];
    u32x2 ps[MAXR];
    {
        const __amdgpu_buffer_rsrc_t rs = __builtin_amdgcn_make_buffer_rsrc((void*)recs, 0, E * 16, 0x00020000);
        const int cnt = c8 < NC ? (int)(ocell >> 16) : 0;
        const int rbase = c8 * CH + (int)(ocell & 0xffffu);
#pragma unroll
        for (int it = 0; it < MAXR; ++it) {
            int r = r8 + 8 * it;
            ra[it] = __builtin_amdgcn_raw_buffer_load_b128(rs, r < cnt ? (rbase + r) * 16 : 0x7ffffff0, 0, 0);
        }
        if (cnt > 8 * MAXR) sMisc[0] = 1;
#pragma unroll
        for (int it = 0; it < MAXR; ++it) {
            bool valid = r8 + 8 * it < cnt;
            ps[it] = pos4[ra[it].y];
            int dl = ra[it].x >> 16;
            key[it] = ((unsigned)dl << 18) | ((unsigned)(c8 * CH) + (ra[it].x & 0xffffu));
            rank[it] = -1;
            if (valid) rank[it] = atomicAdd(&sCnt[dl], 1) | (dl << 16);
        }
    }
    if (NC > NT2 / 8 && tid == 0) sMisc[0] = 1;
    STAMP(1, 7);
    WSTAMP(8);
    if (wave < MT * 4) *(u32x4*)&sH[(tid >> 4) * MST + (tid & 15) * 8] = hv;
    if (tid < T * 3) sPosT[tid] = bf1((unsigned short)posu);
    if (tid < D) sWx[tid] = bf1((unsigned short)wxu) * KIN;
    unsigned sel[2][4];
#pragma unroll
    for (int hf2 = 0; hf2 < 2; ++hf2)
#pragma unroll
        for (int w = 0; w < 4; ++w) {
            int k0 = grp * 8 + w * 2, tk = hf2 * 16 + lg;
            sel[hf2][w] = (k0 == tk ? 0x3f80u : 0u) | (k0 + 1 == tk ? 0x3f800000u : 0u);
        }
    lds_barrier();
    STAMP(1, 5);
    WSTAMP(9);

    int round_pos = 0;
    float* macc = (float*)sWh;
    u32x4* sRaw = (u32x4*)(sKey + POOL + 64);
    u32x2* sRawP = (u32x2*)(sKey + POOL + 64 + POOL * 4);
    int total;
    bool slow;
    {
        int cntl = sCnt[l];
        int incl = wave_incl_scan(cntl);
        int excl = incl - cntl;
        total = __builtin_amdgcn_readlane(incl, 63);
        slow = sMisc[0] != 0 || total > POOL;
        if (!slow) {
#pragma unroll
            for (int it = 0; it < MAXR; ++it) {
                int dl = rank[it] >> 16;
                int o = __builtin_amdgcn_ds_bpermute((dl & 63) << 2, excl);
                if (rank[it] >= 0) {
                    int p = o + (rank[it] & 0xffff);
                    sKey[p] = key[it]; sRaw[p] = ra[it]; sRawP[p] = ps[it];
                }
            }
            if (wave == 0) sOff[l] = excl;
            if (wave == 1 && l < 4) sKey[total + l] = 0xffffffffu;
        }
    }
    STAMP(1, 9);
    auto rank_step = [&](int tid, int total) {
        if (tid < total) {
            u32x4 rr = sRaw[tid];
            u32x2 pp = sRawP[tid];
            unsigned ky = sKey[tid];
            int dl = rr.x >> 16;
            int o = sOff[dl], end = o + sCnt[dl];
            int j0 = o & ~3;
            int p = j0;
            for (int j = j0; j < end; j += 4) {
                u32x4 k4 = *(const u32x4*)&sKey[j];
                p += (k4.x < ky ? 1 : 0) + (k4.y < ky ? 1 : 0) + (k4.z < ky ? 1 : 0) + (k4.w < ky ? 1 : 0);
            }
            float d0 = rbf(sPosT[dl * 3 + 0] - bf_lo(pp.x));
            float d1 = rbf(sPosT[dl * 3 + 1] - bf_hi(pp.x));
            float d2 = rbf(sPosT[dl * 3 + 2] - bf_lo(pp.y));
            float q = rbf(rbf(d0 * d0) + rbf(d1 * d1) + rbf(d2 * d2));
            u32x4 rec;
            rec.x = (__float_as_uint(q) >> 16) | (rr.z << 16);
            rec.y = (rr.z >> 16) | (rr.w << 16);
            rec.z = (rr.w >> 16) | ((unsigned)dl << 16);
            rec.w = rr.y;
            sRec[p] = rec;
            sCf[0][p] = (unsigned short)(__float_as_uint(d0) >> 16);
            sCf[1][p] = (unsigned short)(__float_as_uint(d1) >> 16);
            sCf[2][p] = (unsigned short)(__float_as_uint(d2) >> 16);
            sDl[p] = (unsigned short)dl;
        }
        if (tid < 32) sDl[total + tid] = 0xffffu;
        if (tid >= 64 && tid < 64 + T) {
            int n = tid - 64;
            int o = sOff[n], cnt = sCnt[n];
            sUf[n] = o >> 5;
            sUl[n] = cnt > 0 ? (o + cnt - 1) >> 5 : -1;
        }
    };
    lds_barrier();
    STAMP(1, 10);
    WSTAMP(10);
    if (!slow) rank_step(tid, total);
    WSTAMP(11);
    if (wave >= 12) {
        const int np = wave & 3;
#pragma unroll
        for (int mt = 0; mt < MT; ++mt) {
            f32x4 a0 = {0.f, 0.f, 0.f, 0.f}, a1 = {0.f, 0.f, 0.f, 0.f};
#pragma unroll
            for (int ks = 0; ks < 4; ++ks) {
                bf16x8 hb = *(const bf16x8*)&sH[(mt * 16 + lg) * MST + ks * 32 + grp * 8];
                a0 = __builtin_amdgcn_mfma_f32_16x16x32_bf16(__builtin_bit_cast(bf16x8, w1f[ks]), hb, a0, 0, 0, 0);
                a1 = __builtin_amdgcn_mfma_f32_16x16x32_bf16(__builtin_bit_cast(bf16x8, w1f[4 + ks]), hb, a1, 0, 0, 0);
            }
            int row = mt * 16 + lg;
            if (row < T) {
                u32x4 v;
                v.x = pack2(a0[0] * KSC, a0[1] * KSC); v.y = pack2(a0[2] * KSC, a0[3] * KSC);
                v.z = pack2(a1[0] * KSC, a1[1] * KSC); v.w = pack2(a1[2] * KSC, a1[3] * KSC);
                *(u32x4*)&sA[row * MST + grp * 32 + np * 8] = v;
            }
        }
    }
    if (slow) {
        for (int i = tid; i < T * PST; i += NT2) macc[i] = 0.f;
    } else {
#pragma unroll
        for (int i = 0; i < 4; ++i) {
            unsigned lds_off = __builtin_amdgcn_readfirstlane((unsigned)(uintptr_t)(sWh + i * NT2 + wave * 64));
            const u32x4* gp = frags + 32 * 64 + i * NT2 + tid;
            asm volatile("s_mov_b32 m0, %0\n\ts_nop 0\n\tglobal_load_lds_dwordx4 %1, off" :: "s"(lds_off), "v"(gp) : "memory", "m0");
        }
    }
    STAMP(1, 11);
    WSTAMP(12);
    for (;;) {
        int tid = threadIdx.x;
        asm volatile("" : "+v"(tid));
        const int l = tid & 63;
        const int lg = l & 15, grp = l >> 4;
        if (slow) {
            __syncthreads();
            if (tid < 64) sCnt[tid] = 0;
            if (tid == 0) sMisc[1] = 0;
            __syncthreads();
            int e = round_pos + tid;
            round_pos += NT2;
            int rk = -1, dl = 0;
            u32x4 rr = {0u, 0u, 0u, 0u};
            u32x2 pp = {0u, 0u};
            if (e < E) {
                dl = dst[e] - n0;
                if (dl >= 0 && dl < T) {
                    int s = src[e];
                    u32x2 ea = e_attr[e];
                    rr.x = (unsigned)dl << 16; rr.y = (unsigned)s; rr.z = ea.x; rr.w = ea.y;
                    pp = pos4[s];
                    rk = atomicAdd(&sCnt[dl], 1);
                }
            }
            __syncthreads();
            int cntl = sCnt[l];
            int incl = wave_incl_scan(cntl);
            int excl = incl - cntl;
            total = __builtin_amdgcn_readlane(incl, 63);
            int o = __builtin_amdgcn_ds_bpermute((dl & 63) << 2, excl);
            if (rk >= 0) { int p = o + rk; sKey[p] = ((unsigned)dl << 18) | (unsigned)tid; sRaw[p] = rr; sRawP[p] = pp; }
            if (wave == 0) sOff[l] = excl;
            if (wave == 1 && l < 4) sKey[total + l] = 0xffffffffu;
            __syncthreads();
            rank_step(tid, total);
        }
        lds_barrier();
        STAMP(1, 1);

        const int NW = ((total + 31) >> 5) * 2;
        int wsi = 0;
        WSTAMP(wsi); wsi++;
        auto fetch = [&](int g, u32x4 (&R)[2], u32x4 (&X)[2][2]) {
            int gc = g < NW ? g : NW - 1;
            int e = (gc >> 1) * 32 + lg;
            R[0] = sRec[e < total ? e : total - 1];
            R[1] = sRec[e + 16 < total ? e + 16 : total - 1];
#pragma unroll
            for (int bt = 0; bt < 2; ++bt) {
                const unsigned short* brow = Bt + (size_t)R[bt].w * D + (gc & 1) * 64 + grp * 8;
                X[bt][0] = *(const u32x4*)brow; X[bt][1] = *(const u32x4*)(brow + 32);
            }
        };
        auto body = [&](int g, const u32x4 (&R)[2], const u32x4 (&X)[2][2], u32x4 (&Rn)[2], u32x4 (&Xn)[2][2]) {
            const int u = g >> 1, hg = g & 1;
            const int p0 = u * 32;
            unsigned S[2][4][2];
            fetch(g + 16, Rn, Xn);
            __builtin_amdgcn_sched_barrier(0);
#pragma unroll
            for (int bt = 0; bt < 2; ++bt) {
                const u32x4 rec = R[bt];
                const unsigned short* arow = &sA[(rec.z >> 16) * MST + hg * 64 + grp * 8];
                const u32x4 F = {rec.x, rec.y, rec.z & 0xffffu, 0u};
                f32x4 acc[4];
#pragma unroll
                for (int g4 = 0; g4 < 4; ++g4) {
                    u32x4 Y = *(const u32x4*)(arow + (g4 >> 1) * 32);
                    u32x4 Sl = {sel[g4 & 1][0], sel[g4 & 1][1], sel[g4 & 1][2], sel[g4 & 1][3]};
                    f32x4 a = {0.f, 0.f, 0.f, 0.f};
                    a = __builtin_amdgcn_mfma_f32_16x16x32_bf16(__builtin_bit_cast(bf16x8, F), __builtin_bit_cast(bf16x8, wf[g4]), a, 0, 0, 0);
                    a = __builtin_amdgcn_mfma_f32_16x16x32_bf16(__builtin_bit_cast(bf16x8, Y), __builtin_bit_cast(bf16x8, Sl), a, 0, 0, 0);
                    a = __builtin_amdgcn_mfma_f32_16x16x32_bf16(__builtin_bit_cast(bf16x8, X[bt][g4 >> 1]), __builtin_bit_cast(bf16x8, Sl), a, 0, 0, 0);
                    acc[g4] = a;
                }
#pragma unroll
                for (int g4 = 0; g4 < 4; ++g4) {
#pragma unroll
                    for (int hh = 0; hh < 2; ++hh) {
                        f32x2 x = {acc[g4][hh * 2], acc[g4][hh * 2 + 1]};
                        f32x2 ex = {__builtin_amdgcn_exp2f(x[0]), __builtin_amdgcn_exp2f(x[1])};
                        ex += 1.0f;
                        f32x2 rc = {__builtin_amdgcn_rcpf(ex[0]), __builtin_amdgcn_rcpf(ex[1])};
                        S[bt][g4][hh] = cvt_pk(x * rc);
                    }
                }
                __builtin_amdgcn_sched_barrier(0);
            }
            WSTAMP(wsi); wsi++;
            {
                int l = threadIdx.x;
                asm volatile("" : "+v"(l));
                l &= 63;
                const int lg = l & 15, grp = l >> 4;
                int lastp = p0 + 31 < total ? p0 + 31 : total - 1;
                int nlo = __builtin_amdgcn_readfirstlane((int)sDl[p0]);
                int nhi = __builtin_amdgcn_readfirstlane((int)sDl[lastp]);
                u32x2 dv0 = *(const u32x2*)&sDl[p0 + 4 * grp];
                u32x2 dv1 = *(const u32x2*)&sDl[p0 + 16 + 4 * grp];
                int c = lg & 3;
                int ci = c > 0 ? c - 1 : 0;
                u32x2 cf0 = *(const u32x2*)&sCf[ci][p0 + 4 * grp];
                u32x2 cf1 = *(const u32x2*)&sCf[ci][p0 + 16 + 4 * grp];
                if (c == 0) { cf0 = (u32x2){0x3f803f80u, 0x3f803f80u}; cf1 = cf0; }
                float wxv[4];
#pragma unroll
                for (int j = 0; j < 4; ++j) wxv[j] = sWx[(hg * 4 + j) * 16 + lg];
                for (int base = nlo; base <= nhi; base += 4) {
                    unsigned tg = (unsigned)(base + (lg >> 2)) * 0x10001u;
                    u32x4 G;
                    G.x = eq_sel16(dv0.x, tg, cf0.x); G.y = eq_sel16(dv0.y, tg, cf0.y);
                    G.z = eq_sel16(dv1.x, tg, cf1.x); G.w = eq_sel16(dv1.y, tg, cf1.y);
                    int node = base + grp;
                    bool act = node <= nhi;
                    float* prow = &sProw[(u + node) * PST + hg * 64 + lg];
                    f32x4 M[4];
#pragma unroll
                    for (int j = 0; j < 4; ++j) {
                        u32x4 Sv = {S[0][j][0], S[0][j][1], S[1][j][0], S[1][j][1]};
                        f32x4 z = {0.f, 0.f, 0.f, 0.f};
                        M[j] = __builtin_amdgcn_mfma_f32_16x16x32_bf16(__builtin_bit_cast(bf16x8, G), __builtin_bit_cast(bf16x8, Sv), z, 0, 0, 0);
                    }
                    float q1 = 0.f, q2 = 0.f, q3 = 0.f;
#pragma unroll
                    for (int j = 0; j < 4; ++j) {
                        q1 = fmaf(M[j][1], wxv[j], q1); q2 = fmaf(M[j][2], wxv[j], q2); q3 = fmaf(M[j][3], wxv[j], q3);
                    }
                    q1 = row16_sum(q1); q2 = row16_sum(q2); q3 = row16_sum(q3);
                    if (act) {
#pragma unroll
                        for (int j = 0; j < 4; ++j) prow[j * 16] = M[j][0];
                        if (lg < 3) sPpos[(u + node) * 8 + hg * 4 + lg] = lg == 0 ? q1 : (lg == 1 ? q2 : q3);
                    }
                }
            }
            WSTAMP(wsi); wsi++;
        };
        if (NW > 0) {
            u32x4 RA[2], XA[2][2], RB[2], XB[2][2];
            int g = wave;
            fetch(g, RA, XA);
            __builtin_amdgcn_s_setprio(3);
            for (;;) {
                if (g >= NW) break;
                body(g, RA, XA, RB, XB); g += 16;
                if (g < 32) __builtin_amdgcn_s_setprio(2); else __builtin_amdgcn_s_setprio(0);
                if (g >= NW) break;
                body(g, RB, XB, RA, XA); g += 16;
                if (g < 48) __builtin_amdgcn_s_setprio(1); else __builtin_amdgcn_s_setprio(0);
            }
            __builtin_amdgcn_s_setprio(0);
        }
        STAMP(1, 2);
        asm volatile("s_waitcnt vmcnt(0)" ::: "memory");
        __syncthreads();
        STAMP(1, 3);
        if (!slow) break;
        tid = threadIdx.x;
        asm volatile("" : "+v"(tid));
        for (int i = tid; i < T * 32; i += NT2) {
            int n = i >> 5, c4 = i & 31;
            f32x4 a = *(const f32x4*)&macc[n * PST + c4 * 4];
            for (int uu = sUf[n]; uu <= sUl[n]; ++uu) a += *(const f32x4*)&sProw[(uu + n) * PST + c4 * 4];
            *(f32x4*)&macc[n * PST + c4 * 4] = a;
        }
        if (tid < T * 3) {
            int n = tid / 3, c = tid - n * 3;
            float a = macc[n * PST + D + c];
            for (int uu = sUf[n]; uu <= sUl[n]; ++uu) a += sPpos[(uu + n) * 8 + c] + sPpos[(uu + n) * 8 + 4 + c];
            macc[n * PST + D + c] = a;
        }
        if (round_pos >= E) break;
    }
    if (slow) {
        int tid = threadIdx.x;
        asm volatile("" : "+v"(tid));
        __syncthreads();
        for (int i = tid; i < T * 32; i += NT2) {
            int n = i >> 5, c4 = i & 31;
            *(f32x4*)&sProw[n * PST + c4 * 4] = *(const f32x4*)&macc[n * PST + c4 * 4];
        }
        if (tid < T * 3) { int n = tid / 3, c = tid - n * 3; sPpos[n * 8 + c] = macc[n * PST + D + c]; sPpos[n * 8 + 4 + c] = 0.f; }
        if (tid < T) { sUf[tid] = 0; sUl[tid] = 0; }
        __syncthreads();
#pragma unroll
        for (int i = 0; i < 4; ++i) sWh[i * NT2 + tid] = frags[32 * 64 + i * NT2 + tid];
        __syncthreads();
    }

    if (wave < MT * 4) {
        int l = threadIdx.x;
        asm volatile("" : "+v"(l));
        l &= 63;
        const int lg = l & 15, grp = l >> 4;
        int mt = wave >> 2, np = wave & 3;
        int row = mt * 16 + lg;
        int node = n0 + row;
        bool valid = (row < T) && (node < N);
        int uf = valid ? sUf[row] : 0, ul = valid ? sUl[row] : -1;
        f32x4 a0 = {0.f, 0.f, 0.f, 0.f}, a1 = {0.f, 0.f, 0.f, 0.f};
#pragma unroll
        for (int ks = 0; ks < 8; ++ks) {
            bf16x8 xb;
            if (ks < 4) xb = *(const bf16x8*)&sH[row * MST + ks * 32 + grp * 8];
            else {
                f32x4 m0 = {0.f, 0.f, 0.f, 0.f}, m1 = {0.f, 0.f, 0.f, 0.f};
                for (int uu = uf; uu <= ul; ++uu) {
                    const float* pr = &sProw[(uu + row) * PST + (ks - 4) * 32 + grp * 8];
                    m0 += *(const f32x4*)pr; m1 += *(const f32x4*)(pr + 4);
                }
                u32x4 v;
                m0 *= KIN; m1 *= KIN;
                v.x = cvt_pk((f32x2){m0[0], m0[1]}); v.y = cvt_pk((f32x2){m0[2], m0[3]});
                v.z = cvt_pk((f32x2){m1[0], m1[1]}); v.w = cvt_pk((f32x2){m1[2], m1[3]});
                xb = __builtin_bit_cast(bf16x8, v);
            }
            a0 = __builtin_amdgcn_mfma_f32_16x16x32_bf16(__builtin_bit_cast(bf16x8, sWh[((np * 2) * 8 + ks) * 64 + l]), xb, a0, 0, 0, 0);
            a1 = __builtin_amdgcn_mfma_f32_16x16x32_bf16(__builtin_bit_cast(bf16x8, sWh[((np * 2 + 1) * 8 + ks) * 64 + l]), xb, a1, 0, 0, 0);
        }
        if (valid) {
            u32x4 hv2 = *(const u32x4*)&sH[row * MST + grp * 32 + np * 8];
            u32x4 v;
            v.x = pack2(bf_lo(hv2.x) + silu_f(a0[0]), bf_hi(hv2.x) + silu_f(a0[1]));
            v.y = pack2(bf_lo(hv2.y) + silu_f(a0[2]), bf_hi(hv2.y) + silu_f(a0[3]));
            v.z = pack2(bf_lo(hv2.z) + silu_f(a1[0]), bf_hi(hv2.z) + silu_f(a1[1]));
            v.w = pack2(bf_lo(hv2.w) + silu_f(a1[2]), bf_hi(hv2.w) + silu_f(a1[3]));
            *(u32x4*)(out_h + (size_t)node * D + grp * 32 + np * 8) = v;
        }
    } else {
        int t = threadIdx.x;
        asm volatile("" : "+v"(t));
        t -= MT * 4 * 64;
        if (t < T * 3 && n0 * 3 + t < N * 3) {
            int n = t / 3, c = t - n * 3;
            float a = 0.f;
            for (int uu = sUf[n]; uu <= sUl[n]; ++uu) a += sPpos[(uu + n) * 8 + c] + sPpos[(uu + n) * 8 + 4 + c];
            out_pos[(size_t)n0 * 3 + t] = (unsigned short)f2bf(sPosT[t] + a);
        }
    }
    STAMP(1, 4);
    STAMP_FLUSH(1);
    WSTAMP_FLUSH();
}

extern "C" void kernel_launch(void* const* d_in, const int* in_sizes, int n_in,
                              void* d_out, int out_size, void* d_ws, size_t ws_size,
                              hipStream_t stream) {
    const unsigned short* h = (const unsigned short*)d_in[0];
    const unsigned short* pos = (const unsigned short*)d_in[1];
    const u32x2* e_attr = (const u32x2*)d_in[2];
    const int* src = (const int*)d_in[3];
    const int* dst = (const int*)d_in[4];
    const unsigned short* w_e = (const unsigned short*)d_in[5];
    const unsigned short* w_x = (const unsigned short*)d_in[6];
    const unsigned short* w_h = (const unsigned short*)d_in[7];
    const int N = in_sizes[1] / 3;
    const int E = in_sizes[3];
    const int NB = (N + T - 1) / T;
    const int NC = (E + CH - 1) / CH;
    const int NG = (N + 127) / 128;

    char* ws = (char*)d_ws;
    size_t off = 0;
    auto take = [&](size_t bytes) { char* p = ws + off; off += (bytes + 255) / 256 * 256; return p; };
    u32x4* frags = (u32x4*)take((size_t)104 * 64 * 16);
    unsigned short* Bt = (unsigned short*)take((size_t)N * D * 2);
    u32x2* pos4 = (u32x2*)take((size_t)N * 8);
    u32x4* recs = (u32x4*)take((size_t)NC * CH * 16);
    unsigned* offs = (unsigned*)take((size_t)NC * NB * 4);

    unsigned short* out_h = (unsigned short*)d_out;
    unsigned short* out_pos = out_h + (size_t)N * D;

    size_t lds1 = (size_t)CH * 16 + (size_t)NB * 8;
    if (lds1 < (size_t)2 * 128 * (D + 8) * 2) lds1 = (size_t)2 * 128 * (D + 8) * 2;
    (void)hipFuncSetAttribute((const void*)k1_kernel, hipFuncAttributeMaxDynamicSharedMemorySize, (int)lds1);
    k1_kernel<<<NC + NG + 16, NT1, lds1, stream>>>(h, pos, e_attr, src, dst, w_e, w_h, recs, offs, frags, Bt, pos4,
                                                    N, E, NB, NC, NG);
    k2_kernel<<<NB, NT2, 0, stream>>>(h, pos, e_attr, src, dst, w_e, w_x, recs, offs, frags, Bt, pos4,
                                      out_h, out_pos, N, E, NC, NB);
}
```

```cpp
#include <hip/hip_runtime.h>
#include <hip/hip_bf16.h>
#include <stdint.h>

typedef __attribute__((ext_vector_type(8))) short bf16x8;
typedef __attribute__((ext_vector_type(4))) float f32x4;
typedef __attribute__((ext_vector_type(2))) float f32x2;
typedef __attribute__((ext_vector_type(4))) unsigned u32x4;
typedef __attribute__((ext_vector_type(2))) unsigned u32x2;
typedef __attribute__((ext_vector_type(2))) __bf16 bf16x2v;
typedef __attribute__((ext_vector_type(2))) unsigned short u16x2;

#define STAMP_DECL
#define STAMP(K, i)
#define STAMP_FLUSH(K)
#define WSTAMP(i)
#define WSTAMP_FLUSH()

static constexpr int D = 128;
static constexpr int T = 40;
static constexpr int MT = 3;
static constexpr int POOL = 1024;
static constexpr int CH = 2048;
static constexpr int NT1 = 1024;
static constexpr int NT2 = 1024;

static constexpr float KSC = -1.44269504088896340736f;
static constexpr float KIN = -0.69314718055994530942f;

__device__ __forceinline__ float bf_lo(unsigned u) { return __uint_as_float(u << 16); }
__device__ __forceinline__ float bf_hi(unsigned u) { return __uint_as_float(u & 0xffff0000u); }
__device__ __forceinline__ float bf1(unsigned short u) { return __uint_as_float(((unsigned)u) << 16); }
__device__ __forceinline__ unsigned f2bf(float f) {
    __hip_bfloat16 b = __float2bfloat16(f);
    return (unsigned)*reinterpret_cast<unsigned short*>(&b);
}
__device__ __forceinline__ float rbf(float f) { return __uint_as_float(f2bf(f) << 16); }
__device__ __forceinline__ unsigned pack2(float lo, float hi) { return f2bf(lo) | (f2bf(hi) << 16); }

__device__ __forceinline__ unsigned cvt_pk(f32x2 v) {
    return __builtin_bit_cast(unsigned, __builtin_convertvector(v, bf16x2v));
}
__device__ __forceinline__ unsigned eq_sel16(unsigned a, unsigned b, unsigned c) {
    unsigned d = a ^ b, m;
    asm("v_pk_min_u16 %0, %1, %2" : "=v"(m) : "v"(d), "v"(0x00010001u));
    asm("v_pk_sub_u16 %0, %1, %2" : "=v"(m) : "v"(m), "v"(0x00010001u));
    return m & c;
}
__device__ __forceinline__ void lds_barrier() {
    asm volatile("s_waitcnt lgkmcnt(0)" ::: "memory");
    __builtin_amdgcn_s_barrier();
    asm volatile("" ::: "memory");
}
__device__ __forceinline__ float silu_f(float x) {
    float e = __builtin_amdgcn_exp2f(x * -1.44269504088896340736f);
    return x * __builtin_amdgcn_rcpf(1.0f + e);
}

template <int CTRL> __device__ __forceinline__ float dpp_mov(float v) {
    return __builtin_bit_cast(float, __builtin_amdgcn_update_dpp(0, __builtin_bit_cast(int, v), CTRL, 0xf, 0xf, false));
}
__device__ __forceinline__ float row16_sum(float v) {
    v += dpp_mov<0x128>(v);
    v += dpp_mov<0x124>(v);
    v += dpp_mov<0x122>(v);
    v += dpp_mov<0x121>(v);
    return v;
}
__device__ __forceinline__ int wave_incl_scan(int x) {
    x += __builtin_amdgcn_update_dpp(0, x, 0x111, 0xf, 0xf, false);
    x += __builtin_amdgcn_update_dpp(0, x, 0x112, 0xf, 0xf, false);
    x += __builtin_amdgcn_update_dpp(0, x, 0x114, 0xf, 0xf, false);
    x += __builtin_amdgcn_update_dpp(0, x, 0x118, 0xf, 0xf, false);
    x += __builtin_amdgcn_update_dpp(0, x, 0x142, 0xa, 0xf, false);
    x += __builtin_amdgcn_update_dpp(0, x, 0x143, 0xc, 0xf, false);
    return x;
}

__device__ __forceinline__ int perm_ch(int nt, int i) { return (i >> 2) * 32 + nt * 4 + (i & 3); }

__global__ __launch_bounds__(NT1) void k1_kernel(const unsigned short* __restrict__ h,
                                                 const unsigned short* __restrict__ pos,
                                                 const u32x2* __restrict__ e_attr,
                                                 const int* __restrict__ src, const int* __restrict__ dst,
                                                 const unsigned short* __restrict__ w_e,
                                                 const unsigned short* __restrict__ w_h,
                                                 u32x4* __restrict__ recs, unsigned* __restrict__ offs,
                                                 u32x4* __restrict__ frags,
                                                 unsigned short* __restrict__ Bt, u32x2* __restrict__ pos4,
                                                 int N, int E, int NB, int NC, int NG) {
    extern __shared__ __attribute__((aligned(16))) char smem[];
    const int tid = threadIdx.x;
    const int bid = blockIdx.x;
    STAMP_DECL
    STAMP(0, 0);
    if (bid < NC) {
        u32x4* sRA = (u32x4*)smem;
        int* lcnt = (int*)(smem + CH * 16);
        int* loff = lcnt + NB;
        const int ebase = bid * CH;
        int dd[CH / NT1], ss[CH / NT1]; u32x2 ea[CH / NT1];
#pragma unroll
        for (int it = 0; it < CH / NT1; ++it) {
            int e = ebase + it * NT1 + tid;
            int ec = e < E ? e : E - 1;
            dd[it] = dst[ec]; ss[it] = src[ec]; ea[it] = e_attr[ec];
        }
        for (int i = tid; i < NB; i += NT1) lcnt[i] = 0;
        __syncthreads();
        STAMP(0, 3);
        u32x4 ra[CH / NT1]; int bk[CH / NT1], rk[CH / NT1];
#pragma unroll
        for (int it = 0; it < CH / NT1; ++it) {
            int el = it * NT1 + tid;
            int e = ebase + el;
            bk[it] = -1;
            if (e < E) {
                int d = dd[it];
                int b = d / T;
                int dl = d - b * T;
                rk[it] = atomicAdd(&lcnt[b], 1);
                bk[it] = b;
                ra[it].x = ((unsigned)dl << 16) | (unsigned)el; ra[it].y = (unsigned)ss[it]; ra[it].z = ea[it].x; ra[it].w = ea[it].y;
            }
        }
        __syncthreads();
        STAMP(0, 4);
        if (tid < 64) {
            int carry = 0;
            for (int base = 0; base < NB; base += 256) {
                int i = base + tid * 4;
                int v0 = i < NB ? lcnt[i] : 0, v1 = i + 1 < NB ? lcnt[i + 1] : 0;
                int v2 = i + 2 < NB ? lcnt[i + 2] : 0, v3 = i + 3 < NB ? lcnt[i + 3] : 0;
                int sum = v0 + v1 + v2 + v3;
                int x = wave_incl_scan(sum);
                int e = carry + x - sum;
                if (i < NB) loff[i] = e;
                if (i + 1 < NB) loff[i + 1] = e + v0;
                if (i + 2 < NB) loff[i + 2] = e + v0 + v1;
                if (i + 3 < NB) loff[i + 3] = e + v0 + v1 + v2;
                carry += __builtin_amdgcn_readlane(x, 63);
            }
        }
        __syncthreads();
        STAMP(0, 5);
#pragma unroll
        for (int it = 0; it < CH / NT1; ++it)
            if (bk[it] >= 0) sRA[loff[bk[it]] + rk[it]] = ra[it];
        for (int i = tid; i < NB; i += NT1) offs[(size_t)bid * NB + i] = (unsigned)loff[i] | ((unsigned)lcnt[i] << 16);
        __syncthreads();
        STAMP(0, 1);
        const int nvalid = (E - ebase) < CH ? (E - ebase) : CH;
#pragma unroll
        for (int it = 0; it < CH / NT1; ++it) {
            int p = it * NT1 + tid;
            if (p < nvalid) recs[(size_t)ebase + p] = sRA[p];
        }
        STAMP(0, 2);
        STAMP_FLUSH(0);
        return;
    }
    const int wave = tid >> 6, l = tid & 63;
    const int lg = l & 15, grp = l >> 4;
    if (bid < NC + NG) {
        constexpr int WST = D + 8;
        unsigned short* sW = (unsigned short*)smem;
        unsigned short* sH = sW + 128 * WST;
        const int nb0 = (bid - NC) * 128;
        const u32x4* w2 = (const u32x4*)(w_e + (size_t)128 * D);
        u32x4 wv[2], hv[2];
#pragma unroll
        for (int it = 0; it < 2; ++it) {
            int ch = it * NT1 + tid;
            wv[it] = w2[ch];
            int node = nb0 + (ch >> 4);
            int nodec = node < N ? node : N - 1;
            hv[it] = *(const u32x4*)(h + (size_t)nodec * D + (ch & 15) * 8);
        }
#pragma unroll
        for (int it = 0; it < 2; ++it) {
            int ch = it * NT1 + tid;
            *(u32x4*)&sW[(ch >> 4) * WST + (ch & 15) * 8] = wv[it];
            *(u32x4*)&sH[(ch >> 4) * WST + (ch & 15) * 8] = hv[it];
        }
        __syncthreads();
        STAMP(0, 1);
        const int np = wave & 3, mg = wave >> 2;
        bf16x8 hb[2][4];
#pragma unroll
        for (int m = 0; m < 2; ++m)
#pragma unroll
            for (int ks = 0; ks < 4; ++ks) hb[m][ks] = *(const bf16x8*)&sH[(mg * 32 + m * 16 + lg) * WST + ks * 32 + grp * 8];
        f32x4 acc[2][2];
#pragma unroll
        for (int m = 0; m < 2; ++m) { acc[m][0] = (f32x4){0.f, 0.f, 0.f, 0.f}; acc[m][1] = (f32x4){0.f, 0.f, 0.f, 0.f}; }
#pragma unroll
        for (int q = 0; q < 2; ++q) {
            const unsigned short* wp = &sW[(grp * 8 + (lg >> 2)) * WST + (lg & 3) * 32 + (np * 2 + q) * 4];
#pragma unroll
            for (int ks = 0; ks < 4; ++ks) {
                typedef __attribute__((ext_vector_type(4))) short s16x4;
                typedef __attribute__((address_space(3))) s16x4* lds_s16x4;
                s16x4 lo = __builtin_amdgcn_ds_read_tr16_b64_v4i16((lds_s16x4)(wp + ks * 32 * WST));
                s16x4 hi = __builtin_amdgcn_ds_read_tr16_b64_v4i16((lds_s16x4)(wp + (ks * 32 + 4) * WST));
                bf16x8 wf = {lo[0], lo[1], lo[2], lo[3], hi[0], hi[1], hi[2], hi[3]};
                acc[0][q] = __builtin_amdgcn_mfma_f32_16x16x32_bf16(wf, hb[0][ks], acc[0][q], 0, 0, 0);
                acc[1][q] = __builtin_amdgcn_mfma_f32_16x16x32_bf16(wf, hb[1][ks], acc[1][q], 0, 0, 0);
            }
        }
#pragma unroll
        for (int m = 0; m < 2; ++m) {
            int node = nb0 + mg * 32 + m * 16 + lg;
            if (node < N) {
                u32x4 v;
                v.x = pack2(acc[m][0][0] * KSC, acc[m][0][1] * KSC); v.y = pack2(acc[m][0][2] * KSC, acc[m][0][3] * KSC);
                v.z = pack2(acc[m][1][0] * KSC, acc[m][1][1] * KSC); v.w = pack2(acc[m][1][2] * KSC, acc[m][1][3] * KSC);
                *(u32x4*)(Bt + (size_t)node * D + grp * 32 + np * 8) = v;
            }
        }
        STAMP(0, 2);
        STAMP_FLUSH(0);
        return;
    }
    {
        int t = (bid - NC - NG) * NT1 + tid;
        if (t < 96 * 64) {
            int f = t >> 6;
            const unsigned short* W;
            int nt, ks;
            if (f < 32) { W = w_e; nt = f >> 2; ks = f & 3; }
            else { int g = f - 32; W = w_h; nt = g >> 3; ks = g & 7; }
            int c = perm_ch(nt, lg);
            int k0 = ks * 32 + 8 * grp;
            unsigned v[8];
#pragma unroll
            for (int j = 0; j < 8; ++j) v[j] = W[(size_t)(k0 + j) * D + c];
            u32x4 o;
            o.x = v[0] | (v[1] << 16); o.y = v[2] | (v[3] << 16); o.z = v[4] | (v[5] << 16); o.w = v[6] | (v[7] << 16);
            frags[t] = o;
        } else if (t < 104 * 64) {
            int f = (t >> 6) - 96;
            const unsigned short* wp = w_e + (size_t)256 * D + f * 16 + lg;
            unsigned v[5];
#pragma unroll
            for (int k = 0; k < 5; ++k) {
                float w = bf1(wp[k * D]) * KSC;
                float hi = rbf(w);
                v[k] = grp == 0 ? f2bf(hi) : (grp == 1 ? f2bf(w - hi) : 0u);
            }
            u32x4 o;
            o.x = v[0] | (v[1] << 16); o.y = v[2] | (v[3] << 16); o.z = v[4]; o.w = 0u;
            frags[t] = o;
        } else {
            for (int n = t - 104 * 64; n < N; n += (gridDim.x - NC - NG) * NT1 - 104 * 64) {
                u32x2 o;
                o.x = (unsigned)pos[n * 3] | ((unsigned)pos[n * 3 + 1] << 16);
                o.y = (unsigned)pos[n * 3 + 2];
                pos4[n] = o;
            }
        }
    }
}

static constexpr int MST = D + 8;
static constexpr int PST = D + 16;
static constexpr int NU_MAX = POOL / 32;
static constexpr int NPR = NU_MAX + T;
static constexpr int NGF = NU_MAX + T / 4;
static constexpr int APS = D + 4;

__global__ __launch_bounds__(NT2) void k2_kernel(const unsigned short* __restrict__ h,
                                                 const unsigned short* __restrict__ pos,
                                                 const u32x2* __restrict__ e_attr,
                                                 const int* __restrict__ src, const int* __restrict__ dst,
                                                 const unsigned short* __restrict__ w_e,
                                                 const unsigned short* __restrict__ w_x,
                                                 const u32x4* __restrict__ recs, const unsigned* __restrict__ offs,
                                                 const u32x4* __restrict__ frags, const unsigned short* __restrict__ Bt,
                                                 const u32x2* __restrict__ pos4, float* __restrict__ gacc,
                                                 unsigned short* __restrict__ out_h, unsigned short* __restrict__ out_pos,
                                                 int N, int E, int NC, int NB) {
    __shared__ __attribute__((aligned(16))) float sProw[NPR * PST];
    __shared__ __attribute__((aligned(16))) u32x4 sG[NGF * 64];
    __shared__ __attribute__((aligned(16))) f32x4 sPq[NPR * 32];
    __shared__ __attribute__((aligned(16))) unsigned short sH[MT * 16 * MST];
    __shared__ __attribute__((aligned(16))) unsigned short sA[T * MST];
    __shared__ __attribute__((aligned(16))) u32x4 sRec[POOL];
    __shared__ float sWx[D];
    __shared__ float sPosT[T * 3 + 8];
    __shared__ int sCnt[64], sOff[64];
    __shared__ int sUf[T], sUl[T];
    __shared__ unsigned sUN[NU_MAX];
    __shared__ int sMisc[4];
    unsigned* sKey = (unsigned*)sProw;

    const int tid = threadIdx.x;
    const int wave = __builtin_amdgcn_readfirstlane(tid >> 6);
    const int l = tid & 63;
    const int lg = l & 15, grp = l >> 4;
    const int b = blockIdx.x;
    const int n0 = b * T;
    STAMP_DECL
    STAMP(1, 0);

    constexpr int MAXR = 4;
    const int c8 = tid >> 3, r8 = tid & 7;
    const unsigned ocell = offs[(size_t)(c8 < NC ? c8 : NC - 1) * NB + b];
    u32x4 w1f[8];
    if (wave >= 12) {
#pragma unroll
        for (int i = 0; i < 8; ++i) w1f[i] = frags[((wave & 3) * 8 + i) * 64 + l];
    }
    u32x4 hv = {0u, 0u, 0u, 0u};
    if (wave < MT * 4) {
        int node = n0 + (tid >> 4);
        int nodec = node < N ? node : N - 1;
        hv = *(const u32x4*)(h + (size_t)nodec * D + (tid & 15) * 8);
    }
    unsigned posu, wxu;
    {
        int i = n0 * 3 + (tid < T * 3 ? tid : 0);
        posu = pos[i < N * 3 ? i : N * 3 - 1];
        wxu = w_x[tid & (D - 1)];
    }
    u32x4 wf[4];
#pragma unroll
    for (int i = 0; i < 4; ++i) wf[i] = frags[(96 + (wave & 1) * 4 + i) * 64 + l];
    for (int i = tid; i < NGF * 64; i += NT2) sG[i] = (u32x4){0u, 0u, 0u, 0u};
    if (tid < 64) sCnt[tid] = 0;
    if (tid < 4) sMisc[tid] = 0;
    lds_barrier();
    STAMP(1, 6);

    int rank[MAXR];
    unsigned key[MAXR];
    u32x4 ra[MAXR];
    u32x2 ps[MAXR];
    {
        const __amdgpu_buffer_rsrc_t rs = __builtin_amdgcn_make_buffer_rsrc((void*)recs, 0, E * 16, 0x00020000);
        const int cnt = c8 < NC ? (int)(ocell >> 16) : 0;
        const int rbase = c8 * CH + (int)(ocell & 0xffffu);
#pragma unroll
        for (int it = 0; it < MAXR; ++it) {
            int r = r8 + 8 * it;
            ra[it] = __builtin_amdgcn_raw_buffer_load_b128(rs, r < cnt ? (rbase + r) * 16 : 0x7ffffff0, 0, 0);
        }
        if (cnt > 8 * MAXR) sMisc[0] = 1;
#pragma unroll
        for (int it = 0; it < MAXR; ++it) {
            bool valid = r8 + 8 * it < cnt;
            ps[it] = pos4[ra[it].y];
            int dl = ra[it].x >> 16;
            key[it] = ((unsigned)dl << 18) | ((unsigned)(c8 * CH) + (ra[it].x & 0xffffu));
            rank[it] = -1;
            if (valid) rank[it] = atomicAdd(&sCnt[dl], 1) | (dl << 16);
        }
    }
    if (NC > NT2 / 8 && tid == 0) sMisc[0] = 1;
    STAMP(1, 7);
    WSTAMP(8);
    if (wave < MT * 4) *(u32x4*)&sH[(tid >> 4) * MST + (tid & 15) * 8] = hv;
    if (tid < T * 3) sPosT[tid] = bf1((unsigned short)posu);
    if (tid < D) sWx[tid] = bf1((unsigned short)wxu) * KIN;
    unsigned sel[2][4];
#pragma unroll
    for (int hf2 = 0; hf2 < 2; ++hf2)
#pragma unroll
        for (int w = 0; w < 4; ++w) {
            int k0 = grp * 8 + w * 2, tk = hf2 * 16 + lg;
            sel[hf2][w] = (k0 == tk ? 0x3f80u : 0u) | (k0 + 1 == tk ? 0x3f800000u : 0u);
        }
    lds_barrier();
    STAMP(1, 5);
    WSTAMP(9);

    int round_pos = 0;
    float* macc = gacc + (size_t)b * T * APS;
    u32x4* sRaw = (u32x4*)(sKey + POOL + 64);
    u32x2* sRawP = (u32x2*)(sKey + POOL + 64 + POOL * 4);
    int total;
    bool slow;
    auto node_tables = [&](int n, int o, int cnt, int total) {
        if (n < T) {
            sUf[n] = o >> 5;
            sUl[n] = cnt > 0 ? (o + cnt - 1) >> 5 : -1;
            if (cnt > 0) {
                unsigned short* un = (unsigned short*)sUN;
                for (int u = o >> 5; u <= (o + cnt - 1) >> 5; ++u) {
                    if (u * 32 >= o) un[u * 2] = (unsigned short)n;
                    int last = u * 32 + 31 < total ? u * 32 + 31 : total - 1;
                    if (last < o + cnt) un[u * 2 + 1] = (unsigned short)n;
                }
            }
        }
    };
    auto a_gemm = [&](int mt) {
        const int np = wave & 3;
        f32x4 a0 = {0.f, 0.f, 0.f, 0.f}, a1 = {0.f, 0.f, 0.f, 0.f};
#pragma unroll
        for (int ks = 0; ks < 4; ++ks) {
            bf16x8 hb = *(const bf16x8*)&sH[(mt * 16 + lg) * MST + ks * 32 + grp * 8];
            a0 = __builtin_amdgcn_mfma_f32_16x16x32_bf16(__builtin_bit_cast(bf16x8, w1f[ks]), hb, a0, 0, 0, 0);
            a1 = __builtin_amdgcn_mfma_f32_16x16x32_bf16(__builtin_bit_cast(bf16x8, w1f[4 + ks]), hb, a1, 0, 0, 0);
        }
        int row = mt * 16 + lg;
        if (row < T) {
            u32x4 v;
            v.x = pack2(a0[0] * KSC, a0[1] * KSC); v.y = pack2(a0[2] * KSC, a0[3] * KSC);
            v.z = pack2(a1[0] * KSC, a1[1] * KSC); v.w = pack2(a1[2] * KSC, a1[3] * KSC);
            *(u32x4*)&sA[row * MST + grp * 32 + np * 8] = v;
        }
    };
    if (wave >= 12) a_gemm(0);
    {
        int cntl = sCnt[l];
        int incl = wave_incl_scan(cntl);
        int excl = incl - cntl;
        total = __builtin_amdgcn_readlane(incl, 63);
        slow = sMisc[0] != 0 || total > POOL;
        if (!slow) {
#pragma unroll
            for (int it = 0; it < MAXR; ++it) {
                int dl = rank[it] >> 16;
                int o = __builtin_amdgcn_ds_bpermute((dl & 63) << 2, excl);
                if (rank[it] >= 0) {
                    int p = o + (rank[it] & 0xffff);
                    sKey[p] = key[it]; sRaw[p] = ra[it]; sRawP[p] = ps[it];
                }
            }
            if (wave == 0) { sOff[l] = excl; node_tables(l, excl, cntl, total); }
            if (wave == 1 && l < 20) sKey[total + l] = 0xffffffffu;
        }
    }
    STAMP(1, 9);
    auto rank_step = [&](int tid, int total) {
        if (tid < total) {
            u32x4 rr = sRaw[tid];
            u32x2 pp = sRawP[tid];
            unsigned ky = sKey[tid];
            int dl = rr.x >> 16;
            int o = sOff[dl], end = o + sCnt[dl];
            int nl0 = (int)(sUN[o >> 5] & 0xffffu);
            int j0 = o & ~3;
            int p = j0;
            for (int j = j0; j < end; j += 16) {
                u32x4 k4[4];
#pragma unroll
                for (int q = 0; q < 4; ++q) k4[q] = *(const u32x4*)&sKey[j + q * 4];
#pragma unroll
                for (int q = 0; q < 4; ++q)
                    p += (k4[q].x < ky ? 1 : 0) + (k4[q].y < ky ? 1 : 0) + (k4[q].z < ky ? 1 : 0) + (k4[q].w < ky ? 1 : 0);
            }
            float d0 = rbf(sPosT[dl * 3 + 0] - bf_lo(pp.x));
            float d1 = rbf(sPosT[dl * 3 + 1] - bf_hi(pp.x));
            float d2 = rbf(sPosT[dl * 3 + 2] - bf_lo(pp.y));
            float q = rbf(rbf(d0 * d0) + rbf(d1 * d1) + rbf(d2 * d2));
            u32x4 rec;
            rec.x = (__float_as_uint(q) >> 16) | (rr.z << 16);
            rec.y = (rr.z >> 16) | (rr.w << 16);
            rec.z = (rr.w >> 16) | ((unsigned)(dl * MST * 2) << 16);
            rec.w = rr.y << 8;
            sRec[p] = rec;
            int u = p >> 5;
            int nlo = (u == (o >> 5)) ? nl0 : dl;
            int dd = dl - nlo;
            int gi = u + (nlo >> 2) + (dd >> 2);
            u32x2 ge;
            ge.x = 0x3f80u | (__float_as_uint(d0) & 0xffff0000u);
            ge.y = (__float_as_uint(d1) >> 16) | (__float_as_uint(d2) & 0xffff0000u);
            *(u32x2*)((char*)sG + gi * 1024 + (p & 31) * 32 + (dd & 3) * 8) = ge;
        }
    };
    lds_barrier();
    STAMP(1, 10);
    WSTAMP(10);
    if (!slow) rank_step(tid, total);
    WSTAMP(11);
    if (wave >= 12) { a_gemm(1); a_gemm(2); }
    if (slow) {
        for (int i = tid; i < T * APS; i += NT2) macc[i] = 0.f;
    }
    STAMP(1, 11);
    WSTAMP(12);
    u32x4 whf[16];
    for (;;) {
        int tid = threadIdx.x;
        asm volatile("" : "+v"(tid));
        const int l = tid & 63;
        const int lg = l & 15, grp = l >> 4;
        if (slow) {
            __syncthreads();
            if (tid < 64) sCnt[tid] = 0;
            for (int i = tid; i < NGF * 64; i += NT2) sG[i] = (u32x4){0u, 0u, 0u, 0u};
            __syncthreads();
            int e = round_pos + tid;
            round_pos += NT2;
            int rk = -1, dl = 0;
            u32x4 rr = {0u, 0u, 0u, 0u};
            u32x2 pp = {0u, 0u};
            if (e < E) {
                dl = dst[e] - n0;
                if (dl >= 0 && dl < T) {
                    int s = src[e];
                    u32x2 ea = e_attr[e];
                    rr.x = (unsigned)dl << 16; rr.y = (unsigned)s; rr.z = ea.x; rr.w = ea.y;
                    pp = pos4[s];
                    rk = atomicAdd(&sCnt[dl], 1);
                }
            }
            __syncthreads();
            int cntl = sCnt[l];
            int incl = wave_incl_scan(cntl);
            int excl = incl - cntl;
            total = __builtin_amdgcn_readlane(incl, 63);
            int o = __builtin_amdgcn_ds_bpermute((dl & 63) << 2, excl);
            if (rk >= 0) { int p = o + rk; sKey[p] = ((unsigned)dl << 18) | (unsigned)tid; sRaw[p] = rr; sRawP[p] = pp; }
            if (wave == 0) { sOff[l] = excl; node_tables(l, excl, cntl, total); }
            if (wave == 1 && l < 20) sKey[total + l] = 0xffffffffu;
            __syncthreads();
            rank_step(tid, total);
        }
        lds_barrier();
        STAMP(1, 1);

        const int NW = ((total + 31) >> 5) * 2;
        int wsi = 0;
        WSTAMP(wsi); wsi++;
        const int hg = wave & 1;
        const __amdgpu_buffer_rsrc_t rsB = __builtin_amdgcn_make_buffer_rsrc((void*)Bt, 0, N * (D * 2), 0x00020000);
        const char* sAl = (const char*)sA + hg * 128 + grp * 16;
        float wxv[4];
#pragma unroll
        for (int j = 0; j < 4; ++j) wxv[j] = sWx[(hg * 4 + j) * 16 + lg];
        auto fetch = [&](int g, u32x4 (&R)[2], u32x4 (&X)[2][2]) {
            int gc = g < NW ? g : NW - 1;
            int e = (gc >> 1) * 32 + lg;
            R[0] = sRec[e < total ? e : total - 1];
            R[1] = sRec[e + 16 < total ? e + 16 : total - 1];
#pragma unroll
            for (int bt = 0; bt < 2; ++bt) {
                X[bt][0] = __builtin_amdgcn_raw_buffer_load_b128(rsB, R[bt].w + grp * 16, hg * 128, 0);
                X[bt][1] = __builtin_amdgcn_raw_buffer_load_b128(rsB, R[bt].w + grp * 16 + 64, hg * 128, 0);
            }
        };
        auto body = [&](int g, const u32x4 (&R)[2], const u32x4 (&X)[2][2], u32x4 (&Rn)[2], u32x4 (&Xn)[2][2]) {
            const int u = g >> 1;
            unsigned S[2][4][2];
            const unsigned un = __builtin_amdgcn_readfirstlane(sUN[u]);
            fetch(g + 16, Rn, Xn);
            __builtin_amdgcn_sched_barrier(0);
#pragma unroll
            for (int bt = 0; bt < 2; ++bt) {
                const u32x4 rec = R[bt];
                const char* arow = sAl + (rec.z >> 16);
                f32x4 acc[4];
#pragma unroll
                for (int g4 = 0; g4 < 4; ++g4) {
                    u32x4 Y = *(const u32x4*)(arow + (g4 >> 1) * 64);
                    u32x4 Sl = {sel[g4 & 1][0], sel[g4 & 1][1], sel[g4 & 1][2], sel[g4 & 1][3]};
                    f32x4 a = {0.f, 0.f, 0.f, 0.f};
                    a = __builtin_amdgcn_mfma_f32_16x16x32_bf16(__builtin_bit_cast(bf16x8, rec), __builtin_bit_cast(bf16x8, wf[g4]), a, 0, 0, 0);
                    a = __builtin_amdgcn_mfma_f32_16x16x32_bf16(__builtin_bit_cast(bf16x8, Y), __builtin_bit_cast(bf16x8, Sl), a, 0, 0, 0);
                    a = __builtin_amdgcn_mfma_f32_16x16x32_bf16(__builtin_bit_cast(bf16x8, X[bt][g4 >> 1]), __builtin_bit_cast(bf16x8, Sl), a, 0, 0, 0);
                    acc[g4] = a;
                }
#pragma unroll
                for (int g4 = 0; g4 < 4; ++g4) {
#pragma unroll
                    for (int hh = 0; hh < 2; ++hh) {
                        f32x2 x = {acc[g4][hh * 2], acc[g4][hh * 2 + 1]};
                        f32x2 ex = {__builtin_amdgcn_exp2f(x[0]), __builtin_amdgcn_exp2f(x[1])};
                        ex += 1.0f;
                        f32x2 rc = {__builtin_amdgcn_rcpf(ex[0]), __builtin_amdgcn_rcpf(ex[1])};
                        S[bt][g4][hh] = cvt_pk(x * rc);
                    }
                }
                __builtin_amdgcn_sched_barrier(0);
            }
            WSTAMP(wsi); wsi++;
            {
                const int nlo = un & 0xffffu, nhi = un >> 16;
                const char* gp = (const char*)sG + (u + (nlo >> 2)) * 1024 + l * 8;
                int node = nlo + grp;
                float* prow = &sProw[(u + node) * PST + hg * 64 + lg];
                f32x4* pq = &sPq[(u + node) * 32 + hg * 16 + lg];
                for (int base = nlo; base <= nhi; base += 4) {
                    typedef __attribute__((ext_vector_type(4))) short s16x4;
                    typedef __attribute__((address_space(3))) s16x4* lds_s16x4;
                    s16x4 g0 = __builtin_amdgcn_ds_read_tr16_b64_v4i16((lds_s16x4)gp);
                    s16x4 g1 = __builtin_amdgcn_ds_read_tr16_b64_v4i16((lds_s16x4)(gp + 512));
                    bf16x8 G = {g0[0], g0[1], g0[2], g0[3], g1[0], g1[1], g1[2], g1[3]};
                    f32x4 M[4];
#pragma unroll
                    for (int j = 0; j < 4; ++j) {
                        u32x4 Sv = {S[0][j][0], S[0][j][1], S[1][j][0], S[1][j][1]};
                        f32x4 z = {0.f, 0.f, 0.f, 0.f};
                        M[j] = __builtin_amdgcn_mfma_f32_16x16x32_bf16(G, __builtin_bit_cast(bf16x8, Sv), z, 0, 0, 0);
                    }
                    f32x4 q = {0.f, 0.f, 0.f, 0.f};
#pragma unroll
                    for (int j = 0; j < 4; ++j) {
                        q[0] = fmaf(M[j][1], wxv[j], q[0]); q[1] = fmaf(M[j][2], wxv[j], q[1]); q[2] = fmaf(M[j][3], wxv[j], q[2]);
                    }
                    if (node <= nhi) {
#pragma unroll
                        for (int j = 0; j < 4; ++j) prow[j * 16] = M[j][0];
                        *pq = q;
                    }
                    gp += 1024; node += 4; prow += 4 * PST; pq += 4 * 32;
                }
            }
            WSTAMP(wsi); wsi++;
        };
        if (NW > 0) {
            u32x4 RA[2], XA[2][2], RB[2], XB[2][2];
            int g = wave;
            fetch(g, RA, XA);
            __builtin_amdgcn_s_setprio(3);
            for (;;) {
                if (g >= NW) break;
                body(g, RA, XA, RB, XB); g += 16;
                if (g < 32) __builtin_amdgcn_s_setprio(2); else __builtin_amdgcn_s_setprio(0);
                if (g >= NW) break;
                body(g, RB, XB, RA, XA); g += 16;
                if (g < 48) __builtin_amdgcn_s_setprio(1); else __builtin_amdgcn_s_setprio(0);
            }
            __builtin_amdgcn_s_setprio(0);
        }
        if (wave < MT * 4) {
#pragma unroll
            for (int i = 0; i < 16; ++i) whf[i] = frags[(32 + (wave & 3) * 16 + i) * 64 + l];
        } else {
#pragma unroll
            for (int i = 0; i < 16; ++i) whf[i] = (u32x4){0u, 0u, 0u, 0u};
        }
        STAMP(1, 2);
        lds_barrier();
        STAMP(1, 3);
        if (!slow) break;
        tid = threadIdx.x;
        asm volatile("" : "+v"(tid));
        for (int i = tid; i < T * 32; i += NT2) {
            int n = i >> 5, c4 = i & 31;
            f32x4 a = *(const f32x4*)&macc[n * APS + c4 * 4];
            for (int uu = sUf[n]; uu <= sUl[n]; ++uu) a += *(const f32x4*)&sProw[(uu + n) * PST + c4 * 4];
            *(f32x4*)&macc[n * APS + c4 * 4] = a;
        }
        if (tid < T) {
            int n = tid;
            f32x4 a = *(const f32x4*)&macc[n * APS + D];
            for (int uu = sUf[n]; uu <= sUl[n]; ++uu)
                for (int i = 0; i < 32; ++i) a += sPq[(uu + n) * 32 + i];
            *(f32x4*)&macc[n * APS + D] = a;
        }
        if (round_pos >= E) break;
    }
    if (slow) {
        int tid = threadIdx.x;
        asm volatile("" : "+v"(tid));
        __syncthreads();
        for (int i = tid; i < T * 32; i += NT2) {
            int n = i >> 5, c4 = i & 31;
            *(f32x4*)&sProw[n * PST + c4 * 4] = *(const f32x4*)&macc[n * APS + c4 * 4];
            sPq[i] = c4 == 0 ? *(const f32x4*)&macc[n * APS + D] : (f32x4){0.f, 0.f, 0.f, 0.f};
        }
        if (tid < T) { sUf[tid] = 0; sUl[tid] = 0; }
        __syncthreads();
    }

    if (wave < MT * 4) {
        int l = threadIdx.x;
        asm volatile("" : "+v"(l));
        l &= 63;
        const int lg = l & 15, grp = l >> 4;
        int mt = wave >> 2, np = wave & 3;
        int row = mt * 16 + lg;
        int node = n0 + row;
        bool valid = (row < T) && (node < N);
        int uf = valid ? sUf[row] : 0, ul = valid ? sUl[row] : -1;
        f32x4 a0 = {0.f, 0.f, 0.f, 0.f}, a1 = {0.f, 0.f, 0.f, 0.f};
#pragma unroll
        for (int ks = 0; ks < 8; ++ks) {
            bf16x8 xb;
            if (ks < 4) xb = *(const bf16x8*)&sH[row * MST + ks * 32 + grp * 8];
            else {
                f32x4 m0 = {0.f, 0.f, 0.f, 0.f}, m1 = {0.f, 0.f, 0.f, 0.f};
                for (int uu = uf; uu <= ul; ++uu) {
                    const float* pr = &sProw[(uu + row) * PST + (ks - 4) * 32 + grp * 8];
                    m0 += *(const f32x4*)pr; m1 += *(const f32x4*)(pr + 4);
                }
                u32x4 v;
                m0 *= KIN; m1 *= KIN;
                v.x = cvt_pk((f32x2){m0[0], m0[1]}); v.y = cvt_pk((f32x2){m0[2], m0[3]});
                v.z = cvt_pk((f32x2){m1[0], m1[1]}); v.w = cvt_pk((f32x2){m1[2], m1[3]});
                xb = __builtin_bit_cast(bf16x8, v);
            }
            a0 = __builtin_amdgcn_mfma_f32_16x16x32_bf16(__builtin_bit_cast(bf16x8, whf[ks]), xb, a0, 0, 0, 0);
            a1 = __builtin_amdgcn_mfma_f32_16x16x32_bf16(__builtin_bit_cast(bf16x8, whf[8 + ks]), xb, a1, 0, 0, 0);
        }
        if (valid) {
            u32x4 hv2 = *(const u32x4*)&sH[row * MST + grp * 32 + np * 8];
            u32x4 v;
            v.x = pack2(bf_lo(hv2.x) + silu_f(a0[0]), bf_hi(hv2.x) + silu_f(a0[1]));
            v.y = pack2(bf_lo(hv2.y) + silu_f(a0[2]), bf_hi(hv2.y) + silu_f(a0[3]));
            v.z = pack2(bf_lo(hv2.z) + silu_f(a1[0]), bf_hi(hv2.z) + silu_f(a1[1]));
            v.w = pack2(bf_lo(hv2.w) + silu_f(a1[2]), bf_hi(hv2.w) + silu_f(a1[3]));
            *(u32x4*)(out_h + (size_t)node * D + grp * 32 + np * 8) = v;
        }
    } else {
        int t = threadIdx.x;
        asm volatile("" : "+v"(t));
        t -= MT * 4 * 64;
        int n = t >> 2, qd = t & 3;
        f32x4 a = {0.f, 0.f, 0.f, 0.f};
        if (n < T) {
            for (int uu = sUf[n]; uu <= sUl[n]; ++uu) {
#pragma unroll
                for (int i = 0; i < 8; ++i) a += sPq[(uu + n) * 32 + qd * 8 + i];
            }
        }
#pragma unroll
        for (int c = 0; c < 3; ++c) {
            a[c] += dpp_mov<0xB1>(a[c]);
            a[c] += dpp_mov<0x4E>(a[c]);
        }
        if (n < T && qd < 3 && (n0 + n) < N) {
            float s = qd == 0 ? a[0] : (qd == 1 ? a[1] : a[2]);
            out_pos[(size_t)(n0 + n) * 3 + qd] = (unsigned short)f2bf(sPosT[n * 3 + qd] + s);
        }
    }
    STAMP(1, 4);
    STAMP_FLUSH(1);
    WSTAMP_FLUSH();
}

extern "C" void kernel_launch(void* const* d_in, const int* in_sizes, int n_in,
                              void* d_out, int out_size, void* d_ws, size_t ws_size,
                              hipStream_t stream) {
    const unsigned short* h = (const unsigned short*)d_in[0];
    const unsigned short* pos = (const unsigned short*)d_in[1];
    const u32x2* e_attr = (const u32x2*)d_in[2];
    const int* src = (const int*)d_in[3];
    const int* dst = (const int*)d_in[4];
    const unsigned short* w_e = (const unsigned short*)d_in[5];
    const unsigned short* w_x = (const unsigned short*)d_in[6];
    const unsigned short* w_h = (const unsigned short*)d_in[7];
    const int N = in_sizes[1] / 3;
    const int E = in_sizes[3];
    const int NB = (N + T - 1) / T;
    const int NC = (E + CH - 1) / CH;
    const int NG = (N + 127) / 128;

    char* ws = (char*)d_ws;
    size_t off = 0;
    auto take = [&](size_t bytes) { char* p = ws + off; off += (bytes + 255) / 256 * 256; return p; };
    u32x4* frags = (u32x4*)take((size_t)104 * 64 * 16);
    unsigned short* Bt = (unsigned short*)take((size_t)N * D * 2);
    u32x2* pos4 = (u32x2*)take((size_t)N * 8);
    u32x4* recs = (u32x4*)take((size_t)NC * CH * 16);
    unsigned* offs = (unsigned*)take((size_t)NC * NB * 4);
    float* gacc = (float*)take((size_t)NB * T * APS * 4);

    unsigned short* out_h = (unsigned short*)d_out;
    unsigned short* out_pos = out_h + (size_t)N * D;

    size_t lds1 = (size_t)CH * 16 + (size_t)NB * 8;
    if (lds1 < (size_t)2 * 128 * (D + 8) * 2) lds1 = (size_t)2 * 128 * (D + 8) * 2;
    (void)hipFuncSetAttribute((const void*)k1_kernel, hipFuncAttributeMaxDynamicSharedMemorySize, (int)lds1);
    k1_kernel<<<NC + NG + 16, NT1, lds1, stream>>>(h, pos, e_attr, src, dst, w_e, w_h, recs, offs, frags, Bt, pos4,
                                                    N, E, NB, NC, NG);
    k2_kernel<<<NB, NT2, 0, stream>>>(h, pos, e_attr, src, dst, w_e, w_x, recs, offs, frags, Bt, pos4, gacc,
                                      out_h, out_pos, N, E, NC, NB);
}
```
